```python
import torch

def _softmax(x, axis=-1):
    return torch.softmax(x, dim=axis)
def _gelu(x):
    return torch.nn.functional.gelu(x, approximate="tanh")
def _silu(x):
    return torch.nn.functional.silu(x)
def _sigmoid(x):
    return torch.sigmoid(x)
def _layernorm(x, axis=-1, eps=1e-5):
    m = x.mean(dim=axis, keepdim=True)
    v = x.var(dim=axis, keepdim=True, unbiased=False)
    return (x - m) / torch.sqrt(v + eps)
def _scatter_add(tgt, idx, src, axis=0):
    return tgt.clone().index_add_(axis, idx, src)
def _segment_sum(src, seg, n):
    out = torch.zeros((n,) + src.shape[1:], dtype=src.dtype)
    return out.index_add_(0, seg, src)
def _pad(x, pw):
    flat = tuple(v for pair in reversed(pw) for v in pair)
    return torch.nn.functional.pad(x, flat)

kernel_name = 'parallel_sum__sliding_block_cached__mixer_channel_mlp__decode'

def setup_inputs():

    _w = (lambda a: (lambda t: t.to(torch.bfloat16) if t.is_floating_point() else t)(torch.as_tensor(a)))
    def _A_setup():
        B, Sq, D = 1, 1, 448
        Hq, Hkv, Sk, Dh = 16, 8, 8192, 256
        assert Hq % Hkv == 0, (Hq, Hkv)
        return {
            "x": _w((lambda *s: torch.randn(*s, dtype=torch.bfloat16) * 0.05)(B, Sq, D)),
            "k_cache": _w((lambda *s: torch.randn(*s, dtype=torch.bfloat16) * 3.2)(B, Hkv, Sk, Dh)),
            "v_cache": _w((lambda *s: torch.randn(*s, dtype=torch.bfloat16) * 0.4)(B, Hkv, Sk, Dh)),
            "wq": _w((lambda *s: torch.randn(*s, dtype=torch.bfloat16) * 3.2)(D, Hq * Dh) * (D ** -0.5)),
            "wo": _w((lambda *s: torch.randn(*s, dtype=torch.bfloat16) * 0.4)(Hq * Dh, D) * ((Hq * Dh) ** -0.5)),
        }
    _A_in = _A_setup()

    def _B_setup():
        B, S, D, F = 1, 1, 448, 1792
        return {
            "x": _w((lambda *s: torch.randn(*s, dtype=torch.bfloat16) * 0.05)(B, S, D)),
            "w1": _w((lambda *s: torch.randn(*s, dtype=torch.bfloat16) * 3.2)(D, F) * (D ** -0.5)),
            "b1": _w((lambda *s: torch.randn(*s, dtype=torch.bfloat16) * 1.6)(F)),
            "w2": _w((lambda *s: torch.randn(*s, dtype=torch.bfloat16) * 0.05)(F, D) * (F ** -0.5)),
            "b2": _w((lambda *s: torch.randn(*s, dtype=torch.bfloat16) * 0.05)(D)),
        }
    _B_in = _B_setup()

    return {
        "A_x": _A_in["x"],
        "A_k_cache": _A_in["k_cache"],
        "A_v_cache": _A_in["v_cache"],
        "A_wq": _A_in["wq"],
        "A_wo": _A_in["wo"],
        "B_x": _B_in["x"],
        "B_w1": _B_in["w1"],
        "B_b1": _B_in["b1"],
        "B_w2": _B_in["w2"],
        "B_b2": _B_in["b2"],
    }

def reference(A_x, A_k_cache, A_v_cache, A_wq, A_wo, B_x, B_w1, B_b1, B_w2, B_b2):
    def _A_ref(x, k_cache, v_cache, wq, wo):
        B, Sq, D = x.shape
        Hq, Dh = 16, 256
        Hkv, Sk = k_cache.shape[1], k_cache.shape[2]
        W = min(4096, max(256, Sk // 4))
        rep = Hq // Hkv
        q = (lambda x, *d: x.permute(*d))((x @ wq).reshape(B, Sq, Hq, Dh), 0, 2, 1, 3)
        k = (lambda x, n, axis: torch.repeat_interleave(x, n, dim=axis))(k_cache[:, :, Sk - W:, :], rep, axis=1)
        v = (lambda x, n, axis: torch.repeat_interleave(x, n, dim=axis))(v_cache[:, :, Sk - W:, :], rep, axis=1)
        scores = torch.einsum("bhqd,bhkd->bhqk", q, k) * (Dh ** -0.5)
        p = _softmax(scores, axis=-1)
        o = torch.einsum("bhqk,bhkd->bhqd", p, v)
        return (lambda x, *d: x.permute(*d))(o, 0, 2, 1, 3).reshape(B, Sq, Hq * Dh) @ wo

    def _B_ref(x, w1, b1, w2, b2):
        y = _layernorm(x, axis=-1)
        h = _gelu(torch.einsum("bsd,df->bsf", y, w1) + b1)
        return torch.einsum("bsf,fd->bsd", h, w2) + b2

    x = A_x
    xn = _layernorm(x, axis=-1)
    return x + _A_ref(xn, A_k_cache, A_v_cache, A_wq, A_wo) + _B_ref(xn, B_w1, B_b1, B_w2, B_b2)
```

```cpp
#include <hip/hip_runtime.h>
#include <hip/hip_bf16.h>
#include <stdint.h>

typedef unsigned short u16;
__device__ __forceinline__ float bf2f(u16 v) { return __uint_as_float(((unsigned)v) << 16); }
__device__ __forceinline__ float rbf(float f) {
    unsigned u = __float_as_uint(f); u = (u + 0x7fffu + ((u >> 16) & 1u)) & 0xffff0000u; return __uint_as_float(u);
}
__device__ __forceinline__ u16 f2bf(float f) { return (u16)(__float_as_uint(rbf(f)) >> 16); }

constexpr int D = 448, HQ = 16, HKV = 8, SK = 8192, DH = 256, W = 2048, F = 1792;

__device__ void ln448(float* s, float* red) {
    int t = threadIdx.x;
    float v = 0.f;
    for (int i = t; i < D; i += blockDim.x) v += s[i];
    for (int o = 32; o; o >>= 1) v += __shfl_xor(v, o);
    if ((t & 63) == 0) red[t >> 6] = v;
    __syncthreads();
    float sum = 0.f; for (int i = 0; i < (int)(blockDim.x >> 6); ++i) sum += red[i];
    __syncthreads();
    float mean = sum / D;
    float q = 0.f;
    for (int i = t; i < D; i += blockDim.x) { float d = s[i] - mean; q += d * d; }
    for (int o = 32; o; o >>= 1) q += __shfl_xor(q, o);
    if ((t & 63) == 0) red[t >> 6] = q;
    __syncthreads();
    float qs = 0.f; for (int i = 0; i < (int)(blockDim.x >> 6); ++i) qs += red[i];
    __syncthreads();
    float mb = rbf(mean), vb = rbf(qs / D);
    float den = rbf(sqrtf(rbf(vb + 1e-5f)));
    for (int i = t; i < D; i += blockDim.x) s[i] = rbf(rbf(s[i] - mb) / den);
    __syncthreads();
}

__global__ void k1(const u16* x, const u16* wq, const u16* w1, const u16* b1, float* q, float* h) {
    __shared__ float s[D]; __shared__ float red[16];
    for (int i = threadIdx.x; i < D; i += blockDim.x) s[i] = bf2f(x[i]);
    __syncthreads();
    ln448(s, red);
    int col = blockIdx.x * blockDim.x + threadIdx.x;
    if (blockIdx.x * blockDim.x < HQ * DH) {
        float a = 0.f;
        for (int k = 0; k < D; ++k) a = fmaf(s[k], bf2f(wq[(size_t)k * (HQ * DH) + col]), a);
        q[col] = rbf(a);
    } else {
        ln448(s, red);
        col -= HQ * DH;
        float a = 0.f;
        for (int k = 0; k < D; ++k) a = fmaf(s[k], bf2f(w1[(size_t)k * F + col]), a);
        float u = rbf(rbf(a) + bf2f(b1[col]));
        float g = 0.5f * u * (1.f + tanhf(0.7978845608028654f * (u + 0.044715f * u * u * u)));
        h[col] = rbf(g);
    }
}
__global__ void k2(const float* q, const u16* kc, const u16* vc, float* o) {
    __shared__ float sq[DH]; __shared__ float sc[W]; __shared__ float red[16];
    int hh = blockIdx.x, kv = hh / 2, t = threadIdx.x;
    if (t < DH) sq[t] = q[hh * DH + t];
    __syncthreads();
    const u16* kb = kc + ((size_t)kv * SK + (SK - W)) * DH;
    const u16* vb = vc + ((size_t)kv * SK + (SK - W)) * DH;
    float m = -INFINITY;
    for (int j = t; j < W; j += blockDim.x) {
        float a = 0.f;
        for (int d = 0; d < DH; ++d) a = fmaf(sq[d], bf2f(kb[(size_t)j * DH + d]), a);
        a = rbf(a) * 0.0625f; sc[j] = a; m = fmaxf(m, a);
    }
    for (int of = 32; of; of >>= 1) m = fmaxf(m, __shfl_xor(m, of));
    if ((t & 63) == 0) red[t >> 6] = m;
    __syncthreads();
    for (int i = 0; i < (int)(blockDim.x >> 6); ++i) m = fmaxf(m, red[i]);
    __syncthreads();
    float l = 0.f;
    for (int j = t; j < W; j += blockDim.x) { float e = expf(sc[j] - m); sc[j] = e; l += e; }
    for (int of = 32; of; of >>= 1) l += __shfl_xor(l, of);
    if ((t & 63) == 0) red[t >> 6] = l;
    __syncthreads();
    l = 0.f; for (int i = 0; i < (int)(blockDim.x >> 6); ++i) l += red[i];
    if (t < DH) {
        float a = 0.f;
        for (int j = 0; j < W; ++j) a = fmaf(rbf(sc[j] / l), bf2f(vb[(size_t)j * DH + t]), a);
        o[hh * DH + t] = rbf(a);
    }
}
__global__ void k3(const u16* x, const float* o, const float* h, const u16* wo, const u16* w2, const u16* b2, u16* out) {
    int c = blockIdx.x; int t = threadIdx.x;
    __shared__ float ra[4], rb[4];
    float a = 0.f, b = 0.f;
    for (int k = t; k < HQ * DH; k += blockDim.x) a = fmaf(o[k], bf2f(wo[(size_t)k * D + c]), a);
    for (int k = t; k < F; k += blockDim.x) b = fmaf(h[k], bf2f(w2[(size_t)k * D + c]), b);
    for (int of = 32; of; of >>= 1) { a += __shfl_xor(a, of); b += __shfl_xor(b, of); }
    if ((t & 63) == 0) { ra[t >> 6] = a; rb[t >> 6] = b; }
    __syncthreads();
    if (t == 0) {
        a = ra[0] + ra[1] + ra[2] + ra[3]; b = rb[0] + rb[1] + rb[2] + rb[3];
        float A = rbf(a), B = rbf(rbf(b) + bf2f(b2[c]));
        out[c] = f2bf(rbf(bf2f(x[c]) + A) + B);
    }
}

extern "C" void kernel_launch(void* const* d_in, const int* in_sizes, int n_in,
                              void* d_out, int out_size, void* d_ws, size_t ws_size,
                              hipStream_t stream) {
    const u16* x = (const u16*)d_in[0]; const u16* kc = (const u16*)d_in[1]; const u16* vc = (const u16*)d_in[2];
    const u16* wq = (const u16*)d_in[3]; const u16* wo = (const u16*)d_in[4];
    const u16* w1 = (const u16*)d_in[6]; const u16* b1 = (const u16*)d_in[7]; const u16* w2 = (const u16*)d_in[8]; const u16* b2 = (const u16*)d_in[9];
    float* q = (float*)d_ws; float* h = q + 4096; float* o = h + 2048;
    k1<<<(4096 + 1792) / 64, 64, 0, stream>>>(x, wq, w1, b1, q, h);
    k2<<<16, 1024, 0, stream>>>(q, kc, vc, o);
    k3<<<448, 256, 0, stream>>>(x, o, h, wo, w2, b2, (u16*)d_out);
}
```

```cpp
#include <hip/hip_runtime.h>
#include <hip/hip_bf16.h>
#include <stdint.h>

typedef unsigned short u16;
typedef unsigned u32;
typedef __attribute__((ext_vector_type(4))) unsigned u32x4;
typedef __attribute__((ext_vector_type(2))) unsigned u32x2;
typedef __attribute__((ext_vector_type(4))) float f32x4;
typedef __attribute__((ext_vector_type(2))) float f32x2;
typedef __attribute__((ext_vector_type(8))) __bf16 bf16x8;
typedef __attribute__((ext_vector_type(2))) __bf16 bf16x2;

constexpr int D = 448, SK = 8192, DH = 256, WIN = 2048, F = 1792, NQ = 4096;
constexpr int NWG = 256, NT = 512;
constexpr u32 WS_EPOCH = 0;
constexpr u32 WS_QG = 4096;
constexpr u32 WS_ST = WS_QG + 8 * 2048;
constexpr u32 WS_H2 = 32768;
constexpr u32 WS_H3 = WS_H2 + 8 * 32 * 32 * 128;
constexpr u32 WS_END = WS_H3 + 56 * 256 * 64;
constexpr u32 WS_STAMP = 4u << 20;
constexpr u32 SPIN_CAP = 1u << 16;
constexpr float LOG2E = 1.4426950408889634f;

#define STAMP_WAVES 8
#define STAMP 0

__device__ __forceinline__ float rbf(float f) {
    u32 u = __float_as_uint(f); u = (u + 0x7fffu + ((u >> 16) & 1u)) & 0xffff0000u; return __uint_as_float(u);
}
__device__ __forceinline__ float bflo(u32 v) { return __uint_as_float(v << 16); }
__device__ __forceinline__ float bfhi(u32 v) { return __uint_as_float(v & 0xffff0000u); }
__device__ __forceinline__ u32 pk2(float lo, float hi) {
    return __builtin_amdgcn_perm(__float_as_uint(hi), __float_as_uint(lo), 0x07060302);
}
__device__ __forceinline__ float dot2(u32 a, u32 b, float c) {
    return __builtin_amdgcn_fdot2_f32_bf16(__builtin_bit_cast(bf16x2, a), __builtin_bit_cast(bf16x2, b), c, false);
}
template <int CTRL> __device__ __forceinline__ float dpp(float x) {
    return __builtin_bit_cast(float, __builtin_amdgcn_mov_dpp(__builtin_bit_cast(int, x), CTRL, 0xf, 0xf, true));
}
constexpr int XOR1 = 0xB1, XOR2 = 0x4E, XOR7 = 0x141, XOR8 = 0x128, ROR4 = 0x124;
__device__ __forceinline__ float x16_sum(float x) {
    auto s = __builtin_amdgcn_permlane16_swap(__float_as_uint(x), __float_as_uint(x), false, false);
    return __uint_as_float(s[0]) + __uint_as_float(s[1]);
}
__device__ __forceinline__ float x32_sum(float x) {
    auto s = __builtin_amdgcn_permlane32_swap(__float_as_uint(x), __float_as_uint(x), false, false);
    return __uint_as_float(s[0]) + __uint_as_float(s[1]);
}
__device__ __forceinline__ float x16_max(float x) {
    auto s = __builtin_amdgcn_permlane16_swap(__float_as_uint(x), __float_as_uint(x), false, false);
    return fmaxf(__uint_as_float(s[0]), __uint_as_float(s[1]));
}
__device__ __forceinline__ float x32_max(float x) {
    auto s = __builtin_amdgcn_permlane32_swap(__float_as_uint(x), __float_as_uint(x), false, false);
    return fmaxf(__uint_as_float(s[0]), __uint_as_float(s[1]));
}
__device__ __forceinline__ float row16_sum(float x) {
    x += dpp<XOR1>(x); x += dpp<XOR2>(x); x += dpp<XOR7>(x); x += dpp<XOR8>(x); return x;
}
__device__ __forceinline__ float row16_max(float x) {
    x = fmaxf(x, dpp<XOR1>(x)); x = fmaxf(x, dpp<XOR2>(x)); x = fmaxf(x, dpp<XOR7>(x)); x = fmaxf(x, dpp<XOR8>(x)); return x;
}
__device__ __forceinline__ float rdl(float x, int l) { return __uint_as_float((u32)__builtin_amdgcn_readlane((int)__float_as_uint(x), l)); }
__device__ __forceinline__ float wave_sum(float x) { return x32_sum(x16_sum(row16_sum(x))); }

__device__ __forceinline__ u32x4 ldg16(const void* p) { return *reinterpret_cast<const u32x4*>(p); }
__device__ __forceinline__ u32x4 ldg16nt(const void* p) { return __builtin_nontemporal_load(reinterpret_cast<const u32x4*>(p)); }
#define LDS_FENCE() asm volatile("s_waitcnt lgkmcnt(0)" ::: "memory")
#define BARRIER() do { LDS_FENCE(); __builtin_amdgcn_s_barrier(); asm volatile("" ::: "memory"); } while (0)

#define TS(i) do { } while (0)

struct Smem {
    u32 q[256];
    float part[4][512];
    float statm[2][4];
    float statl[2][4];
    float hs[8];
    float red[8][448];
    float fin[8][8];
};

__global__ void __launch_bounds__(NT, 2)
fused_decode(const u16* __restrict__ x, const u16* __restrict__ kc, const u16* __restrict__ vc,
             const u16* __restrict__ wq, const u16* __restrict__ wo, const u16* __restrict__ w1,
             const u16* __restrict__ b1, const u16* __restrict__ w2, const u16* __restrict__ b2,
             u16* __restrict__ out, unsigned char* ws) {
    __shared__ __attribute__((aligned(16))) Smem sm;
    const int tid = threadIdx.x, lane = tid & 63;
    const int wave = __builtin_amdgcn_readfirstlane(tid >> 6);
    const int b = blockIdx.x, h = b & 7, s = b >> 3;
    TS(0);
    const __amdgpu_buffer_rsrc_t wsr = __builtin_amdgcn_make_buffer_rsrc(ws, 0, WS_END, 0x00020000);
    asm volatile("" :: "s"(x), "s"(kc), "s"(vc), "s"(wq), "s"(wo), "s"(w1), "s"(b1), "s"(w2), "s"(b2), "s"(out), "s"(ws));
    u32 eoff = WS_EPOCH;
    asm volatile("" : "+v"(eoff));
    const u32 epoch = __builtin_amdgcn_raw_buffer_load_b32(wsr, eoff, 0, 16);
    const int lc = lane < 56 ? lane : 55;
    const bool has_mlp = b < 224;
    const int r16 = lane & 15, g = lane >> 4;

    u32x4 woA, woB, w2r;
    f32x4 o0 = {0.f, 0.f, 0.f, 0.f}, o1 = {0.f, 0.f, 0.f, 0.f};
    auto load_tail_weights = [&]() {
        const u16* wop = wo + (size_t)(h * 512 + s * 16 + 2 * wave) * D + 8 * lc;
        woA = ldg16(wop); woB = ldg16(wop + D);
        w2r = ldg16(w2 + (size_t)((has_mlp ? b : 0) * 8 + wave) * D + 8 * lc);
    };
    u32 tag;

    if (wave < 4) {
        const size_t key0 = (size_t)h * SK + (SK - WIN) + s * 64 + wave * 16;
        const u16* kp = kc + (key0 + r16) * DH + 8 * g;
        const u16* vp = vc + (key0 + 4 * g) * DH + 8 * r16;
        u32x4 kf[8], vf[8];
#pragma unroll
        for (int t = 0; t < 8; ++t) kf[t] = ldg16(kp + 32 * t);
#pragma unroll
        for (int t = 0; t < 4; ++t) { vf[2 * t] = ldg16(vp + t * DH); vf[2 * t + 1] = ldg16(vp + t * DH + 128); }
        load_tail_weights();
        tag = (epoch + 1u) * 0x9E3779B1u ^ 0x5BD1E995u;
        u32 mk[4];
#pragma unroll
        for (int d = 0; d < 4; ++d) mk[d] = (((lane & 7) >> 1) == d) ? ((lane & 1) ? 0xffff0000u : 0x0000ffffu) : 0u;
        BARRIER();
        TS(3);
        f32x4 sc = {0.f, 0.f, 0.f, 0.f};
        const u32* qp = sm.q + (r16 >> 3) * 128 + 4 * g;
#pragma unroll
        for (int t = 0; t < 8; ++t) {
            const u32x4 qf = *reinterpret_cast<const u32x4*>(qp + 16 * t);
            sc = __builtin_amdgcn_mfma_f32_16x16x32_bf16(__builtin_bit_cast(bf16x8, kf[t]), __builtin_bit_cast(bf16x8, qf), sc, 0, 0, 0);
        }
        float z[4];
#pragma unroll
        for (int i = 0; i < 4; ++i) z[i] = rbf(sc[i]) * 0.0625f;
        float m = fmaxf(fmaxf(z[0], z[1]), fmaxf(z[2], z[3]));
        m = x32_max(x16_max(m));
        float p[4], l = 0.f;
#pragma unroll
        for (int i = 0; i < 4; ++i) { p[i] = __builtin_amdgcn_exp2f((z[i] - m) * LOG2E); l += p[i]; }
        l = x32_sum(x16_sum(l));
        TS(5);
#pragma unroll
        for (int t = 0; t < 4; ++t) {
            const float pr = rbf(p[t]);
            const u32 pp = pk2(pr, pr);
            const u32x4 af = {pp & mk[0], pp & mk[1], pp & mk[2], pp & mk[3]};
            o0 = __builtin_amdgcn_mfma_f32_16x16x32_bf16(__builtin_bit_cast(bf16x8, af), __builtin_bit_cast(bf16x8, vf[2 * t]), o0, 0, 0, 0);
            o1 = __builtin_amdgcn_mfma_f32_16x16x32_bf16(__builtin_bit_cast(bf16x8, af), __builtin_bit_cast(bf16x8, vf[2 * t + 1]), o1, 0, 0, 0);
        }
        float* pw = &sm.part[wave][(g >> 1) * 256 + 8 * r16 + 4 * (g & 1)];
        *reinterpret_cast<f32x4*>(pw) = o0;
        *reinterpret_cast<f32x4*>(pw + 128) = o1;
        if ((lane & 55) == 0) { sm.statm[lane >> 3][wave] = m; sm.statl[lane >> 3][wave] = l; }
        TS(6);
    } else if (wave < 7) {
        const bool isq = wave < 6;
        const u32x4 xv = ldg16(x + 8 * lc);
        const u16* wp = isq ? wq + (size_t)(8 * lc) * NQ + (h * 512 + s * 16 + (wave - 4) * 8)
                            : w1 + (size_t)(8 * lc) * F + (has_mlp ? b : 0) * 8;
        const int rs = isq ? NQ : F;
        u32x4 wf[8];
#pragma unroll
        for (int i = 0; i < 8; ++i) wf[i] = ldg16(wp + (size_t)i * rs);
        const u32 b1v = b1[(has_mlp ? b : 0) * 8 + (lane & 7)];
        load_tail_weights();
        tag = (epoch + 1u) * 0x9E3779B1u ^ 0x5BD1E995u;
        float v[8];
#pragma unroll
        for (int d = 0; d < 4; ++d) { v[2 * d] = bflo(xv[d]); v[2 * d + 1] = bfhi(xv[d]); }
        const float valid = lane < 56 ? 1.f : 0.f;
        const int nln = isq ? 1 : 2;
        for (int it = 0; it < nln; ++it) {
            float sum = ((v[0] + v[1]) + (v[2] + v[3])) + ((v[4] + v[5]) + (v[6] + v[7]));
            const float mean = wave_sum(sum * valid) * (1.f / D);
            float ss = 0.f;
#pragma unroll
            for (int j = 0; j < 8; ++j) { const float d = v[j] - mean; ss = fmaf(d, d, ss); }
            const float var = wave_sum(ss * valid) * (1.f / D);
            const float mb = rbf(mean);
            const float rden = __builtin_amdgcn_rcpf(rbf(__builtin_sqrtf(rbf(rbf(var) + 1e-5f))));
#pragma unroll
            for (int j = 0; j < 8; ++j) v[j] = rbf(rbf(v[j] - mb) * rden);
        }
        TS(1);
        u32 yp[4];
#pragma unroll
        for (int k = 0; k < 4; ++k) yp[k] = lane < 56 ? pk2(v[2 * k], v[2 * k + 1]) : 0u;
        float a[8];
#pragma unroll
        for (int j = 0; j < 8; ++j) a[j] = 0.f;
#pragma unroll
        for (int k = 0; k < 4; ++k)
#pragma unroll
            for (int d = 0; d < 4; ++d) {
                const u32 lo = __builtin_amdgcn_perm(wf[2 * k + 1][d], wf[2 * k][d], 0x05040100);
                const u32 hi = __builtin_amdgcn_perm(wf[2 * k + 1][d], wf[2 * k][d], 0x07060302);
                a[2 * d] = dot2(lo, yp[k], a[2 * d]);
                a[2 * d + 1] = dot2(hi, yp[k], a[2 * d + 1]);
            }
#pragma unroll
        for (int j = 0; j < 4; ++j) { const float t1 = a[j] + dpp<XOR7>(a[j]), t2 = a[j + 4] + dpp<XOR7>(a[j + 4]); a[j] = (lane & 4) ? t2 : t1; }
#pragma unroll
        for (int j = 0; j < 2; ++j) { const float t1 = a[j] + dpp<XOR2>(a[j]), t2 = a[j + 2] + dpp<XOR2>(a[j + 2]); a[j] = (lane & 2) ? t2 : t1; }
        { const float t1 = a[0] + dpp<XOR1>(a[0]), t2 = a[1] + dpp<XOR1>(a[1]); a[0] = (lane & 1) ? t2 : t1; }
        float r = a[0]; r += dpp<XOR8>(r); r = x32_sum(x16_sum(r));
        r = rbf(r);
        if (isq) {
            const float nb = dpp<XOR1>(r);
            const u32x2 gr = {pk2(r, nb), tag};
            if ((lane & 57) == 0)
                __builtin_amdgcn_raw_buffer_store_b64(gr, wsr, WS_QG + h * 2048 + (s * 8 + (wave - 4) * 4 + (lane >> 1)) * 8, 0, 16);
        } else {
            const float u = rbf(r + bflo(b1v));
            const float zz = 0.7978845608028654f * (u + 0.044715f * u * u * u);
            const float th = 1.f - 2.f * __builtin_amdgcn_rcpf(__builtin_amdgcn_exp2f(2.f * LOG2E * zz) + 1.f);
            const float hv = rbf(0.5f * u * (1.f + th));
            if (lane < 8) sm.hs[lane] = has_mlp ? hv : 0.f;
        }
        TS(2);
        BARRIER();
    } else {
        load_tail_weights();
        tag = (epoch + 1u) * 0x9E3779B1u ^ 0x5BD1E995u;
        u32x4 qa, qb;
#pragma nounroll
        for (u32 spin = 0; spin < SPIN_CAP; ++spin) {
            asm volatile("" ::: "memory");
            qa = __builtin_amdgcn_raw_buffer_load_b128(wsr, WS_QG + h * 2048 + lane * 32, 0, 16);
            qb = __builtin_amdgcn_raw_buffer_load_b128(wsr, WS_QG + h * 2048 + lane * 32 + 16, 0, 16);
            const bool ok = (qa[1] == tag) & (qa[3] == tag) & (qb[1] == tag) & (qb[3] == tag);
            if (__all(ok)) break;
            __builtin_amdgcn_s_sleep(1);
        }
        const u32x4 qq = {qa[0], qa[2], qb[0], qb[2]};
        *reinterpret_cast<u32x4*>(sm.q + 4 * lane) = qq;
        TS(3);
        BARRIER();
    }
    BARRIER();
    if (wave < 4) {
        const int head = tid >> 7, d0 = 2 * (tid & 127);
        const f32x4 mm = *reinterpret_cast<const f32x4*>(sm.statm[head]);
        const f32x4 ll = *reinterpret_cast<const f32x4*>(sm.statl[head]);
        const float M = fmaxf(fmaxf(mm[0], mm[1]), fmaxf(mm[2], mm[3]));
        float a0 = 0.f, a1 = 0.f, L = 0.f;
#pragma unroll
        for (int w = 0; w < 4; ++w) {
            const float e = __builtin_amdgcn_exp2f((mm[w] - M) * LOG2E);
            const f32x2 pv = *reinterpret_cast<const f32x2*>(&sm.part[w][head * 256 + d0]);
            a0 = fmaf(e, pv[0], a0); a1 = fmaf(e, pv[1], a1); L = fmaf(e, ll[w], L);
        }
        const int f = head * 256 + d0;
        const u32x4 gr = {__float_as_uint(a0), tag, __float_as_uint(a1), tag};
        __builtin_amdgcn_raw_buffer_store_b128(gr, wsr, WS_H2 + ((h * 32 + (f >> 4)) * 32 + s) * 128 + (f & 15) * 8, 0, 16);
        if ((tid & 127) == 0) {
            const u32x4 gs = {__float_as_uint(M), tag, __float_as_uint(L), tag};
            __builtin_amdgcn_raw_buffer_store_b128(gs, wsr, WS_ST + ((h * 2 + head) * 32 + s) * 16, 0, 16);
        }
    }
    TS(7);
    float acc[8];
    {
        const float hw = sm.hs[wave];
#pragma unroll
        for (int d = 0; d < 4; ++d) { acc[2 * d] = hw * bflo(w2r[d]); acc[2 * d + 1] = hw * bfhi(w2r[d]); }
    }
    {
        const u32 off = lane < 32 ? WS_H2 + ((h * 32 + s) * 32 + lane) * 128 + wave * 16
                                  : WS_ST + ((h * 2 + (s >> 4)) * 32 + (lane - 32)) * 16;
        u32x4 gv;
#pragma nounroll
        for (u32 spin = 0; spin < SPIN_CAP; ++spin) {
            asm volatile("" ::: "memory");
            gv = __builtin_amdgcn_raw_buffer_load_b128(wsr, off, 0, 16);
            const bool ok = (gv[1] == tag) & (gv[3] == tag);
            if (__all(ok)) break;
            __builtin_amdgcn_s_sleep(1);
        }
        TS(8);
        const float g0 = __uint_as_float(gv[0]), g1 = __uint_as_float(gv[2]);
        float M = x16_max(row16_max(g0));
        const float e = __builtin_amdgcn_exp2f((g0 - M) * LOG2E);
        auto sw = __builtin_amdgcn_permlane32_swap(__float_as_uint(e), __float_as_uint(e), false, false);
        const float eu = __uint_as_float(sw[1]);
        float a0 = eu * g0, a1 = eu * g1;
        a0 = x16_sum(row16_sum(a0)); a1 = x16_sum(row16_sum(a1));
        const float A0 = rdl(a0, 0), A1 = rdl(a1, 0), L = rdl(a1, 32);
        const float inv = __builtin_amdgcn_rcpf(L);
        const float oa = rbf(A0 * inv), ob = rbf(A1 * inv);
#pragma unroll
        for (int d = 0; d < 4; ++d) {
            acc[2 * d] = fmaf(oa, bflo(woA[d]), acc[2 * d]); acc[2 * d + 1] = fmaf(oa, bfhi(woA[d]), acc[2 * d + 1]);
            acc[2 * d] = fmaf(ob, bflo(woB[d]), acc[2 * d]); acc[2 * d + 1] = fmaf(ob, bfhi(woB[d]), acc[2 * d + 1]);
        }
        if (lane < 56) {
            *reinterpret_cast<f32x4*>(&sm.red[wave][8 * lane]) = f32x4{acc[0], acc[1], acc[2], acc[3]};
            *reinterpret_cast<f32x4*>(&sm.red[wave][8 * lane + 4]) = f32x4{acc[4], acc[5], acc[6], acc[7]};
        }
    }
    BARRIER();
    if (tid < 224) {
        float c0 = 0.f, c1 = 0.f;
#pragma unroll
        for (int w = 0; w < 8; ++w) { const f32x2 v = *reinterpret_cast<const f32x2*>(&sm.red[w][2 * tid]); c0 += v[0]; c1 += v[1]; }
        const u32x4 gr = {__float_as_uint(c0), tag, __float_as_uint(c1), tag};
        __builtin_amdgcn_raw_buffer_store_b128(gr, wsr, WS_H3 + ((tid >> 2) * 256 + b) * 64 + (tid & 3) * 16, 0, 16);
    }
    TS(9);
    if (b < 56) {
        const float xb = (tid < 8) ? bflo(x[8 * b + tid]) + bflo(b2[8 * b + tid]) : 0.f;
        const u32 off = WS_H3 + (b * 256 + wave * 32) * 64 + lane * 16;
        u32x4 ga, gb;
#pragma nounroll
        for (u32 spin = 0; spin < SPIN_CAP; ++spin) {
            asm volatile("" ::: "memory");
            ga = __builtin_amdgcn_raw_buffer_load_b128(wsr, off, 0, 16);
            gb = __builtin_amdgcn_raw_buffer_load_b128(wsr, off + 1024, 0, 16);
            const bool ok = (ga[1] == tag) & (ga[3] == tag) & (gb[1] == tag) & (gb[3] == tag);
            if (__all(ok)) break;
            __builtin_amdgcn_s_sleep(1);
        }
        TS(10);
        float c0 = __uint_as_float(ga[0]) + __uint_as_float(gb[0]);
        float c1 = __uint_as_float(ga[2]) + __uint_as_float(gb[2]);
        c0 += dpp<ROR4>(c0); c1 += dpp<ROR4>(c1);
        c0 += dpp<XOR8>(c0); c1 += dpp<XOR8>(c1);
        c0 = x32_sum(x16_sum(c0)); c1 = x32_sum(x16_sum(c1));
        if (lane < 4) *reinterpret_cast<f32x2*>(&sm.fin[wave][2 * lane]) = f32x2{c0, c1};
        BARRIER();
        if (tid < 8) {
            float r = xb;
#pragma unroll
            for (int w = 0; w < 8; ++w) r += sm.fin[w][tid];
            out[8 * b + tid] = (u16)(__float_as_uint(rbf(r)) >> 16);
            if (b == 0 && tid == 0) __hip_atomic_store((u32*)(ws + WS_EPOCH), epoch + 1u, __ATOMIC_RELAXED, __HIP_MEMORY_SCOPE_AGENT);
        }
    }
    TS(11);
}

extern "C" void kernel_launch(void* const* d_in, const int* in_sizes, int n_in,
                              void* d_out, int out_size, void* d_ws, size_t ws_size,
                              hipStream_t stream) {
    fused_decode<<<NWG, NT, 0, stream>>>((const u16*)d_in[0], (const u16*)d_in[1], (const u16*)d_in[2],
                                         (const u16*)d_in[3], (const u16*)d_in[4], (const u16*)d_in[6],
                                         (const u16*)d_in[7], (const u16*)d_in[8], (const u16*)d_in[9],
                                         (u16*)d_out, (unsigned char*)d_ws);
}
```

```cpp
#include <hip/hip_runtime.h>
#include <hip/hip_bf16.h>
#include <stdint.h>

typedef unsigned short u16;
typedef unsigned u32;
typedef __attribute__((ext_vector_type(4))) unsigned u32x4;
typedef __attribute__((ext_vector_type(2))) unsigned u32x2;
typedef __attribute__((ext_vector_type(4))) float f32x4;
typedef __attribute__((ext_vector_type(2))) float f32x2;
typedef __attribute__((ext_vector_type(8))) __bf16 bf16x8;
typedef __attribute__((ext_vector_type(2))) __bf16 bf16x2;

constexpr int D = 448, SK = 8192, DH = 256, WIN = 2048, F = 1792, NQ = 4096;
constexpr int NWG = 256, NT = 448;
constexpr u32 WS_EPOCH = 0;
constexpr u32 WS_QG = 4096;
constexpr u32 WS_HG = WS_QG + 8 * 2048;
constexpr u32 WS_ST = 32768;
constexpr u32 WS_H2 = 65536;
constexpr u32 WS_H3 = WS_H2 + 8 * 32 * 32 * 128;
constexpr u32 WS_END = WS_H3 + 56 * 256 * 64;
constexpr u32 WS_STAMP = 4u << 20;
constexpr u32 SPIN_CAP = 1u << 16;
constexpr float LOG2E = 1.4426950408889634f;
constexpr int AUX_NT = 2, AUX_SC1 = 16;

#define STAMP_WAVES 7
#define STAMP 0

__device__ __forceinline__ u32 cvt2(float lo, float hi) {
    const f32x2 v = {lo, hi};
    return __builtin_bit_cast(u32, __builtin_convertvector(v, bf16x2));
}
__device__ __forceinline__ float bflo(u32 v) { return __uint_as_float(v << 16); }
__device__ __forceinline__ float bfhi(u32 v) { return __uint_as_float(v & 0xffff0000u); }
__device__ __forceinline__ float rbf(float f) { return bfhi(cvt2(f, f)); }
__device__ __forceinline__ float dot2(u32 a, u32 b, float c) {
    return __builtin_amdgcn_fdot2_f32_bf16(__builtin_bit_cast(bf16x2, a), __builtin_bit_cast(bf16x2, b), c, false);
}
template <int CTRL> __device__ __forceinline__ float dpp(float x) {
    return __builtin_bit_cast(float, __builtin_amdgcn_mov_dpp(__builtin_bit_cast(int, x), CTRL, 0xf, 0xf, true));
}
constexpr int XOR1 = 0xB1, XOR2 = 0x4E, XOR7 = 0x141, ROR8 = 0x128, ROR4 = 0x124;
__device__ __forceinline__ float sw16_add(float a, float b) {
    auto s = __builtin_amdgcn_permlane16_swap(__float_as_uint(a), __float_as_uint(b), false, false);
    return __uint_as_float(s[0]) + __uint_as_float(s[1]);
}
__device__ __forceinline__ float sw32_add(float a, float b) {
    auto s = __builtin_amdgcn_permlane32_swap(__float_as_uint(a), __float_as_uint(b), false, false);
    return __uint_as_float(s[0]) + __uint_as_float(s[1]);
}
__device__ __forceinline__ float x16_max(float x) {
    auto s = __builtin_amdgcn_permlane16_swap(__float_as_uint(x), __float_as_uint(x), false, false);
    return fmaxf(__uint_as_float(s[0]), __uint_as_float(s[1]));
}
__device__ __forceinline__ float x32_max(float x) {
    auto s = __builtin_amdgcn_permlane32_swap(__float_as_uint(x), __float_as_uint(x), false, false);
    return fmaxf(__uint_as_float(s[0]), __uint_as_float(s[1]));
}
__device__ __forceinline__ float row16_sum(float x) {
    x += dpp<XOR1>(x); x += dpp<XOR2>(x); x += dpp<XOR7>(x); x += dpp<ROR8>(x); return x;
}
__device__ __forceinline__ float row16_max(float x) {
    x = fmaxf(x, dpp<XOR1>(x)); x = fmaxf(x, dpp<XOR2>(x)); x = fmaxf(x, dpp<XOR7>(x)); x = fmaxf(x, dpp<ROR8>(x)); return x;
}
__device__ __forceinline__ float wave_sum(float x) { x = row16_sum(x); x = sw16_add(x, x); return sw32_add(x, x); }
__device__ __forceinline__ f32x4 mfma(u32x4 a, u32x4 b, f32x4 c) {
    return __builtin_amdgcn_mfma_f32_16x16x32_bf16(__builtin_bit_cast(bf16x8, a), __builtin_bit_cast(bf16x8, b), c, 0, 0, 0);
}
typedef __amdgpu_buffer_rsrc_t rsrc_t;
__device__ __forceinline__ rsrc_t mkrs(const void* p) { return __builtin_amdgcn_make_buffer_rsrc(const_cast<void*>(p), 0, 0x7fffffff, 0x00020000); }
__device__ __forceinline__ u32x4 ldnt(rsrc_t r, u32 voff, u32 soff) { return __builtin_amdgcn_raw_buffer_load_b128(r, voff, soff, AUX_NT); }
__device__ __forceinline__ u32x4 ldsc(rsrc_t r, u32 voff) { return __builtin_amdgcn_raw_buffer_load_b128(r, voff, 0, AUX_SC1); }

#define CBAR() asm volatile("" ::: "memory")
#define LDS_FENCE() asm volatile("s_waitcnt lgkmcnt(0)" ::: "memory")
#define BARRIER() do { LDS_FENCE(); __builtin_amdgcn_s_barrier(); CBAR(); } while (0)

#define TS(i) do { } while (0)

enum { F_G = 0, F_Q, F_S0, F_S1, F_V, F_H2, F_H, F_N };
struct Smem {
    u32 q[256];
    float sc[2][2][64][4];
    float gp[4][32];
    u32 hs[16];
    float fin[4][8];
    u32 flag[8];
};
typedef __attribute__((address_space(3))) u32 lds_u32;
__device__ __forceinline__ void lds_wait(u32* f, u32 want) {
    volatile lds_u32* p = (volatile lds_u32*)f;
#pragma nounroll
    for (u32 i = 0; i < (1u << 22); ++i) { if (__builtin_amdgcn_readfirstlane(*p) >= want) break; }
    CBAR();
}
__device__ __forceinline__ void lds_add(u32* f, int lane) {
    LDS_FENCE();
    if (lane == 0) __hip_atomic_fetch_add((lds_u32*)f, 1u, __ATOMIC_RELAXED, __HIP_MEMORY_SCOPE_WORKGROUP);
    CBAR();
}

__global__ void __launch_bounds__(NT, 2)
fused_decode(const u16* __restrict__ x, const u16* __restrict__ kc, const u16* __restrict__ vc,
             const u16* __restrict__ wq, const u16* __restrict__ wo, const u16* __restrict__ w1,
             const u16* __restrict__ b1, const u16* __restrict__ w2, const u16* __restrict__ b2,
             u16* __restrict__ out, unsigned char* ws) {
    __shared__ __attribute__((aligned(16))) Smem sm;
    const int tid = threadIdx.x, lane = tid & 63;
    const int wave = __builtin_amdgcn_readfirstlane(tid >> 6);
    const int b = blockIdx.x, h = b & 7, s = b >> 3;
    TS(0);
    const unsigned long long tstart = __builtin_amdgcn_s_memrealtime();
    asm volatile("" :: "s"(x), "s"(kc), "s"(vc), "s"(wq), "s"(wo), "s"(w1), "s"(b1), "s"(w2), "s"(b2), "s"(out), "s"(ws));
    const rsrc_t wsr = __builtin_amdgcn_make_buffer_rsrc(ws, 0, WS_END, 0x00020000);
    const int r16 = lane & 15, g = lane >> 4;
    const bool is_m2 = (s >= 23) & (s < 30);
    u32 eoff = WS_EPOCH;
    asm volatile("" : "+v"(eoff));
#define LOAD_EPOCH() __builtin_amdgcn_raw_buffer_load_b32(wsr, eoff, 0, AUX_SC1)
#define MK_TAG(e) ((u32)__builtin_amdgcn_readfirstlane((e) + 1u) * 0x9E3779B1u ^ 0x5BD1E995u)
    u32 mk[4];
#pragma unroll
    for (int d = 0; d < 4; ++d) mk[d] = (((lane & 7) >> 1) == d) ? ((lane & 1) ? 0xffff0000u : 0x0000ffffu) : 0u;

    if (wave < 4) {
        u32 tag;
        const u32 xr = x[8 * (b < 56 ? b : 0) + (lane & 7)], b2r = b2[8 * (b < 56 ? b : 0) + (lane & 7)];
        if (s < 23) {
            const bool isq = s < 16;
            const u32x4 xv = *reinterpret_cast<const u32x4*>(x + 8 * (lane < 56 ? lane : 55));
            const u32 epoch = LOAD_EPOCH();
            const int srcl = 16 * wave + (lane >> 2);
            const rsrc_t wr = mkrs(isq ? wq + (h * 512 + s * 32) : w1 + (h * 7 + s - 16) * 32);
            const u32 rs = isq ? NQ * 2 : F * 2;
            const u32 voff = (u32)(srcl < 56 ? srcl : 55) * 8u * rs + (lane & 3) * 16;
            u32x4 wf[8];
#pragma unroll
            for (int i = 0; i < 8; ++i) wf[i] = ldnt(wr, voff, i * rs);
            const u32 b1v = *reinterpret_cast<const u32*>(b1 + (isq ? 0 : (h * 7 + s - 16) * 32) + 2 * r16);
            CBAR();
            __builtin_amdgcn_s_barrier();
            CBAR();
            TS(1);
            u32 xp[4] = {xv[0], xv[1], xv[2], xv[3]};
            const float valid = lane < 56 ? 1.f : 0.f;
            const int nln = isq ? 1 : 2;
#pragma nounroll
            for (int it = 0; it < nln; ++it) {
                float v[8];
#pragma unroll
                for (int d = 0; d < 4; ++d) { v[2 * d] = bflo(xp[d]); v[2 * d + 1] = bfhi(xp[d]); }
                const float sum = ((v[0] + v[1]) + (v[2] + v[3])) + ((v[4] + v[5]) + (v[6] + v[7]));
                const float mean = wave_sum(sum * valid) * (1.f / D);
                float ss = 0.f;
#pragma unroll
                for (int j = 0; j < 8; ++j) { const float d = v[j] - mean; ss = fmaf(d, d, ss); }
                const float var = wave_sum(ss * valid) * (1.f / D);
                const float mb = rbf(mean);
                const float rden = __builtin_amdgcn_rcpf(rbf(__builtin_sqrtf(rbf(rbf(var) + 1e-5f))));
#pragma unroll
                for (int d = 0; d < 4; ++d) {
                    const u32 c = cvt2(v[2 * d] - mb, v[2 * d + 1] - mb);
                    xp[d] = cvt2(bflo(c) * rden, bfhi(c) * rden);
                }
            }
            u32 yp[4];
#pragma unroll
            for (int d = 0; d < 4; ++d) yp[d] = __builtin_amdgcn_ds_bpermute(srcl * 4, lane < 56 ? xp[d] : 0u);
            TS(2);
            float a[8];
#pragma unroll
            for (int j = 0; j < 8; ++j) a[j] = 0.f;
#pragma unroll
            for (int k = 0; k < 4; ++k)
#pragma unroll
                for (int d = 0; d < 4; ++d) {
                    const u32 lo = __builtin_amdgcn_perm(wf[2 * k + 1][d], wf[2 * k][d], 0x05040100);
                    const u32 hi = __builtin_amdgcn_perm(wf[2 * k + 1][d], wf[2 * k][d], 0x07060302);
                    a[2 * d] = dot2(lo, yp[k], a[2 * d]);
                    a[2 * d + 1] = dot2(hi, yp[k], a[2 * d + 1]);
                }
#pragma unroll
            for (int j = 0; j < 4; ++j) a[j] = sw32_add(a[j], a[j + 4]);
            a[0] = sw16_add(a[0], a[2]); a[1] = sw16_add(a[1], a[3]);
            a[0] += dpp<ROR8>(a[0]); a[1] += dpp<ROR8>(a[1]);
            a[0] += dpp<ROR4>(a[0]); a[1] += dpp<ROR4>(a[1]);
            if ((lane & 12) == 0) *reinterpret_cast<f32x2*>(&sm.gp[wave][8 * (lane & 3) + 2 * g]) = f32x2{a[0], a[1]};
            tag = MK_TAG(epoch);
            LDS_FENCE();
            u32 old = 0;
            if (lane == 0) old = __hip_atomic_fetch_add((lds_u32*)&sm.flag[F_G], 1u, __ATOMIC_RELAXED, __HIP_MEMORY_SCOPE_WORKGROUP);
            TS(3);
            if (__builtin_amdgcn_readfirstlane(old) == 3) {
                CBAR();
                f32x2 r = *reinterpret_cast<const f32x2*>(&sm.gp[0][2 * r16]);
                r += *reinterpret_cast<const f32x2*>(&sm.gp[1][2 * r16]);
                r += *reinterpret_cast<const f32x2*>(&sm.gp[2][2 * r16]);
                r += *reinterpret_cast<const f32x2*>(&sm.gp[3][2 * r16]);
                u32 pv = cvt2(r[0], r[1]);
                if (!isq) {
                    const u32 uu = cvt2(bflo(pv) + bflo(b1v), bfhi(pv) + bfhi(b1v));
                    float hv[2];
#pragma unroll
                    for (int e = 0; e < 2; ++e) {
                        const float u = e ? bfhi(uu) : bflo(uu);
                        const float zz = 0.7978845608028654f * (u + 0.044715f * u * u * u);
                        const float th = 1.f - 2.f * __builtin_amdgcn_rcpf(__builtin_amdgcn_exp2f(2.f * LOG2E * zz) + 1.f);
                        hv[e] = 0.5f * u * (1.f + th);
                    }
                    pv = cvt2(hv[0], hv[1]);
                }
                const u32x2 gr = {pv, tag};
                const u32 base = isq ? WS_QG + h * 2048 + s * 128 : WS_HG + (h * 7 + s - 16) * 128;
                if (lane < 16) __builtin_amdgcn_raw_buffer_store_b64(gr, wsr, base + lane * 8, 0, AUX_SC1);
                TS(4);
            }
        } else {
            const u32 epoch = LOAD_EPOCH();
            CBAR();
            __builtin_amdgcn_s_barrier();
            CBAR();
            tag = MK_TAG(epoch);
        }
        lds_add(&sm.flag[F_H2], lane);
        const int cc = wave == 3 ? (r16 & 7) : r16;
        const u32 coff = (u32)(128 * wave + 8 * cc) * 2;
        lds_wait(&sm.flag[F_V], 2);
        TS(5);
        u32x4 wof[4], w2f[8];
        {
            const rsrc_t wr = mkrs(wo + (size_t)(h * 512 + s * 16) * D);
#pragma unroll
            for (int t = 0; t < 4; ++t) wof[t] = ldnt(wr, coff + g * (4 * D * 2), t * D * 2);
        }
        f32x4 acc = {0.f, 0.f, 0.f, 0.f};
        if (is_m2) {
            const rsrc_t wr = mkrs(w2 + (size_t)(h * 7 + s - 23) * 32 * D);
#pragma unroll
            for (int t = 0; t < 8; ++t) w2f[t] = ldnt(wr, coff + g * (8 * D * 2), t * D * 2);
            lds_wait(&sm.flag[F_H], 1);
            const u32x4 hv = *reinterpret_cast<const u32x4*>(&sm.hs[4 * g]);
#pragma unroll
            for (int t = 0; t < 8; ++t) {
                const u32 pp = __builtin_amdgcn_perm(hv[t >> 1], hv[t >> 1], (t & 1) ? 0x03020302 : 0x01000100);
                acc = mfma(u32x4{pp & mk[0], pp & mk[1], pp & mk[2], pp & mk[3]}, w2f[t], acc);
            }
        }
        lds_wait(&sm.flag[F_H2], 6);
        TS(6);
        {
            const u32 doff = WS_H2 + ((h * 32 + s) * 32 + r16) * 128 + g * 16;
            const u32 soff = WS_ST + ((h * 2 + (s >> 4)) * 32 + r16) * 16;
            u32x4 d00, d01, d10, d11, s0, s1;
#pragma nounroll
            for (u32 spin = 0; spin < SPIN_CAP; ++spin) {
                CBAR();
                s0 = ldsc(wsr, soff); s1 = ldsc(wsr, soff + 256);
                d00 = ldsc(wsr, doff); d01 = ldsc(wsr, doff + 64);
                d10 = ldsc(wsr, doff + 2048); d11 = ldsc(wsr, doff + 2048 + 64);
                const bool ok = (s0[1] == tag) & (s0[3] == tag) & (s1[1] == tag) & (s1[3] == tag) &
                                (d00[1] == tag) & (d00[3] == tag) & (d01[1] == tag) & (d01[3] == tag) &
                                (d10[1] == tag) & (d10[3] == tag) & (d11[1] == tag) & (d11[3] == tag);
                if (__all(ok)) break;
            }
            TS(7);
            const float m0 = __uint_as_float(s0[0]), m1 = __uint_as_float(s1[0]);
            const float M = row16_max(fmaxf(m0, m1));
            const float e0 = __builtin_amdgcn_exp2f((m0 - M) * LOG2E), e1 = __builtin_amdgcn_exp2f((m1 - M) * LOG2E);
            const float L = row16_sum(fmaf(e0, __uint_as_float(s0[2]), e1 * __uint_as_float(s1[2])));
            const float inv = __builtin_amdgcn_rcpf(L);
            float o[4];
            o[0] = fmaf(e0, __uint_as_float(d00[0]), e1 * __uint_as_float(d10[0]));
            o[1] = fmaf(e0, __uint_as_float(d00[2]), e1 * __uint_as_float(d10[2]));
            o[2] = fmaf(e0, __uint_as_float(d01[0]), e1 * __uint_as_float(d11[0]));
            o[3] = fmaf(e0, __uint_as_float(d01[2]), e1 * __uint_as_float(d11[2]));
#pragma unroll
            for (int t = 0; t < 4; ++t) {
                const float ot = row16_sum(o[t]) * inv;
                const u32 pp = cvt2(ot, ot);
                acc = mfma(u32x4{pp & mk[0], pp & mk[1], pp & mk[2], pp & mk[3]}, wof[t], acc);
            }
            const float c0 = g < 2 ? acc[0] : acc[2], c1 = g < 2 ? acc[1] : acc[3];
            const u32x4 gr = {__float_as_uint(c0), tag, __float_as_uint(c1), tag};
            if (16 * wave + r16 < 56)
                __builtin_amdgcn_raw_buffer_store_b128(gr, wsr, WS_H3 + ((16 * wave + r16) * 256 + b) * 64 + g * 16, 0, AUX_SC1);
        }
        TS(8);
        if (b < 56) {
            const u32 off = WS_H3 + (b * 256 + wave * 64) * 64 + lane * 16;
            u32x4 ga, gb, gc, gd;
#pragma nounroll
            for (u32 spin = 0; spin < SPIN_CAP; ++spin) {
                CBAR();
                ga = ldsc(wsr, off); gb = ldsc(wsr, off + 1024); gc = ldsc(wsr, off + 2048); gd = ldsc(wsr, off + 3072);
                const bool ok = (ga[1] == tag) & (ga[3] == tag) & (gb[1] == tag) & (gb[3] == tag) &
                                (gc[1] == tag) & (gc[3] == tag) & (gd[1] == tag) & (gd[3] == tag);
                if (__all(ok)) break;
            }
            TS(9);
            const float c0 = (__uint_as_float(ga[0]) + __uint_as_float(gb[0])) + (__uint_as_float(gc[0]) + __uint_as_float(gd[0]));
            const float c1 = (__uint_as_float(ga[2]) + __uint_as_float(gb[2])) + (__uint_as_float(gc[2]) + __uint_as_float(gd[2]));
            float c = sw32_add(c0, c1);
            c = sw16_add(c, c);
            c += dpp<ROR8>(c); c += dpp<ROR4>(c);
            if ((lane & 28) == 0) sm.fin[wave][4 * (lane & 1) + (lane & 2) + (lane >> 5)] = c;
            BARRIER();
            if (tid < 8) {
                const float r = (bflo(xr) + bflo(b2r)) + ((sm.fin[0][tid] + sm.fin[1][tid]) + (sm.fin[2][tid] + sm.fin[3][tid]));
                out[8 * b + tid] = (u16)(cvt2(r, r) >> 16);
                if (b == 0 && tid == 0) {
                    const u32 ep1 = (tag ^ 0x5BD1E995u) * 0x0E8B2F51u;
                    __hip_atomic_store((u32*)(ws + WS_EPOCH), ep1, __ATOMIC_RELAXED, __HIP_MEMORY_SCOPE_AGENT);
                }
            }
            TS(10);
        }
    } else if (wave < 6) {
        const int kw = wave - 4;
        const u32 epoch = LOAD_EPOCH();
        const size_t base = ((size_t)h * SK + (SK - WIN) + s * 64) * DH;
        const rsrc_t kr = mkrs(kc + base + kw * 32 * DH), vr = mkrs(vc + base + kw * 128);
        const u32 koff = (r16 >> 2) * 512 + (4 * g + (r16 & 3)) * 16;
        const u32 voff = g * 512 + r16 * 16;
        CBAR();
        __builtin_amdgcn_s_barrier();
        CBAR();
        TS(1);
        u32x4 kf[16], vf[16];
#pragma unroll
        for (int a = 0; a < 8; ++a) { kf[2 * a] = ldnt(kr, koff, a * 2048); kf[2 * a + 1] = ldnt(kr, koff + 256, a * 2048); }
        TS(2);
#pragma unroll
        for (int t = 0; t < 16; ++t) vf[t] = ldnt(vr, voff, t * 2048);
        lds_add(&sm.flag[F_V], lane);
        TS(3);
        const u32 tag = MK_TAG(epoch);
        lds_wait(&sm.flag[F_Q], 1);
        TS(4);
        const u32* qp = sm.q + (r16 >> 3) * 128 + (4 * g + (r16 & 3)) * 4;
        const u32x4 q0 = *reinterpret_cast<const u32x4*>(qp), q1 = *reinterpret_cast<const u32x4*>(qp + 64);
        float z[16];
#pragma unroll
        for (int a = 0; a < 8; ++a) {
            f32x4 d = {0.f, 0.f, 0.f, 0.f};
            d = mfma(kf[2 * a], q0, d);
            d = mfma(kf[2 * a + 1], q1, d);
            const float t01 = (lane & 1) ? d[1] : d[0], t23 = (lane & 1) ? d[3] : d[2];
            float v = (lane & 2) ? t23 : t01;
            v += dpp<XOR1>(v); v += dpp<XOR2>(v);
            z[a] = rbf(v) * 0.0625f;
        }
        *reinterpret_cast<f32x4*>(sm.sc[kw][0][lane]) = f32x4{z[0], z[1], z[2], z[3]};
        *reinterpret_cast<f32x4*>(sm.sc[kw][1][lane]) = f32x4{z[4], z[5], z[6], z[7]};
        lds_add(&sm.flag[F_S0], lane);
        TS(5);
        lds_wait(&sm.flag[F_S0], 2);
        {
            const f32x4 a0 = *reinterpret_cast<const f32x4*>(sm.sc[0][0][lane]), a1 = *reinterpret_cast<const f32x4*>(sm.sc[0][1][lane]);
            const f32x4 a2 = *reinterpret_cast<const f32x4*>(sm.sc[1][0][lane]), a3 = *reinterpret_cast<const f32x4*>(sm.sc[1][1][lane]);
#pragma unroll
            for (int i = 0; i < 4; ++i) { z[i] = a0[i]; z[4 + i] = a1[i]; z[8 + i] = a2[i]; z[12 + i] = a3[i]; }
        }
        float m = z[0];
#pragma unroll
        for (int i = 1; i < 16; ++i) m = fmaxf(m, z[i]);
        m = x32_max(x16_max(m));
        float l = 0.f;
        u32x4 af[16];
#pragma unroll
        for (int i = 0; i < 16; ++i) {
            const float p = __builtin_amdgcn_exp2f((z[i] - m) * LOG2E);
            l += p;
            const u32 pp = cvt2(p, p);
            af[i] = u32x4{pp & mk[0], pp & mk[1], pp & mk[2], pp & mk[3]};
        }
        l = sw16_add(l, l); l = sw32_add(l, l);
        if (lane == 8 * kw) {
            const u32x4 gs = {__float_as_uint(m), tag, __float_as_uint(l), tag};
            __builtin_amdgcn_raw_buffer_store_b128(gs, wsr, WS_ST + ((h * 2 + kw) * 32 + s) * 16, 0, AUX_SC1);
        }
        TS(6);
        f32x4 o = {0.f, 0.f, 0.f, 0.f};
#pragma unroll
        for (int t = 0; t < 16; ++t) o = mfma(af[t], vf[t], o);
        const u32 hoff = WS_H2 + ((h * 32 + (g >> 1) * 16 + kw * 8 + (r16 >> 1)) * 32 + s) * 128 + (2 * (r16 & 1) + (g & 1)) * 16;
        __builtin_amdgcn_raw_buffer_store_b128(u32x4{__float_as_uint(o[0]), tag, __float_as_uint(o[1]), tag}, wsr, hoff, 0, AUX_SC1);
        __builtin_amdgcn_raw_buffer_store_b128(u32x4{__float_as_uint(o[2]), tag, __float_as_uint(o[3]), tag}, wsr, hoff + 64, 0, AUX_SC1);
        lds_add(&sm.flag[F_H2], lane);
        TS(7);
    } else {
        if (lane < 8) sm.flag[lane] = 0;
        const u32 epoch = LOAD_EPOCH();
        BARRIER();
        __builtin_amdgcn_s_setprio(3);
        const u32 tag = MK_TAG(epoch);
        TS(1);
        u32x4 qa, qb;
        u32 np = 0;
#pragma nounroll
        for (u32 spin = 0; spin < SPIN_CAP; ++spin) {
            CBAR();
            qa = ldsc(wsr, WS_QG + h * 2048 + lane * 32);
            qb = ldsc(wsr, WS_QG + h * 2048 + lane * 32 + 16);
            ++np;
            const bool ok = (qa[1] == tag) & (qa[3] == tag) & (qb[1] == tag) & (qb[3] == tag);
            if (__all(ok)) break;
            __builtin_amdgcn_s_sleep(2);
        }
        *reinterpret_cast<u32x4*>(sm.q + 4 * lane) = u32x4{qa[0], qa[2], qb[0], qb[2]};
        lds_add(&sm.flag[F_Q], lane);
        TS(2);
        if (is_m2) {
            u32x4 hv;
#pragma nounroll
            for (u32 spin = 0; spin < SPIN_CAP; ++spin) {
                CBAR();
                hv = ldsc(wsr, WS_HG + (h * 7 + s - 23) * 128 + (lane & 7) * 16);
                const bool ok = (hv[1] == tag) & (hv[3] == tag);
                if (__all(ok)) break;
                __builtin_amdgcn_s_sleep(2);
            }
            if (lane < 8) *reinterpret_cast<u32x2*>(sm.hs + 2 * lane) = u32x2{hv[0], hv[2]};
            lds_add(&sm.flag[F_H], lane);
            TS(3);
        }
    }
    while (__builtin_amdgcn_s_memrealtime() - tstart < 1100ull) __builtin_amdgcn_s_sleep(1);
}

extern "C" void kernel_launch(void* const* d_in, const int* in_sizes, int n_in,
                              void* d_out, int out_size, void* d_ws, size_t ws_size,
                              hipStream_t stream) {
    fused_decode<<<NWG, NT, 0, stream>>>((const u16*)d_in[0], (const u16*)d_in[1], (const u16*)d_in[2],
                                         (const u16*)d_in[3], (const u16*)d_in[4], (const u16*)d_in[6],
                                         (const u16*)d_in[7], (const u16*)d_in[8], (const u16*)d_in[9],
                                         (u16*)d_out, (unsigned char*)d_ws);
}
```

```cpp
#include <hip/hip_runtime.h>
#include <hip/hip_bf16.h>
#include <stdint.h>

typedef unsigned short u16;
typedef unsigned u32;
typedef __attribute__((ext_vector_type(4))) unsigned u32x4;
typedef __attribute__((ext_vector_type(2))) unsigned u32x2;
typedef __attribute__((ext_vector_type(8))) unsigned u32x8;
typedef __attribute__((ext_vector_type(16))) unsigned u32x16;
typedef __attribute__((ext_vector_type(4))) float f32x4;
typedef __attribute__((ext_vector_type(2))) float f32x2;
typedef __attribute__((ext_vector_type(8))) __bf16 bf16x8;
typedef __attribute__((ext_vector_type(2))) __bf16 bf16x2;

constexpr int D = 448, SK = 8192, DH = 256, WIN = 2048, F = 1792, NQ = 4096;
constexpr int NWG = 256, NT = 448;
constexpr u32 WS_EPOCH = 0;
constexpr u32 WS_ST = 4096;
constexpr u32 WS_QG = 32768;
constexpr u32 WS_U = WS_QG + 8 * 16384;
constexpr u32 WS_H2 = WS_U + 224 * 1024;
constexpr u32 WS_H3 = WS_H2 + 8 * 32 * 32 * 64;
constexpr u32 WS_END = WS_H3 + 56 * 256 * 64;
constexpr u32 WS_STAMP = 4u << 20;
constexpr u32 SPIN_CAP = 1u << 16;
constexpr float LOG2E = 1.4426950408889634f;
constexpr int AUX_NT = 2, AUX_SC1 = 16;

#define STAMP_WAVES 7
#define STAMP 0
#define W1T 85
#define KT 95
#define KP 210
#define VT 235
#define VP 210
#define TAILT 375
#define TP 300
#define ST 430
#define QDELAY 14
#define ROTA (s + h)
#define ROTH ((s >> 3) & 1)
#define PACE(i, P) do { if (((i) + 1) * (P) / 64 - (i) * (P) / 64 > (P) / 64) __builtin_amdgcn_s_sleep((P) / 64 + 1); else if ((P) >= 64) __builtin_amdgcn_s_sleep((P) / 64); CBAR(); } while (0)
#define H2T 500
#define H2DELAY 0
#define H3DELAY 14
#define RETRY 1
#define PFSPLIT 4
#define H2PAIR 1
#define PREWARM 0
#define STAMP_CLASS(ss, wv) (wv == 0 ? 0 : wv == 2 ? (ss < 25 ? 1 : 2) : wv == 6 ? 4 : wv >= 4 ? 3 : 9)
#define STAMP_NAMES {"V0 ", "T0 ", "T0F", "K  ", "P  "}
#define DUMPW0 0
#define DUMPWS 2
#define STR_(x) #x
#define STR(x) STR_(x)

__device__ __forceinline__ u32 cvt2(float lo, float hi) {
    const f32x2 v = {lo, hi};
    return __builtin_bit_cast(u32, __builtin_convertvector(v, bf16x2));
}
__device__ __forceinline__ float bflo(u32 v) { return __uint_as_float(v << 16); }
__device__ __forceinline__ float bfhi(u32 v) { return __uint_as_float(v & 0xffff0000u); }
__device__ __forceinline__ float rbf(float f) { return bfhi(cvt2(f, f)); }
__device__ __forceinline__ float dot2(u32 a, u32 b, float c) {
    return __builtin_amdgcn_fdot2_f32_bf16(__builtin_bit_cast(bf16x2, a), __builtin_bit_cast(bf16x2, b), c, false);
}
template <int CTRL> __device__ __forceinline__ float dpp(float x) {
    return __builtin_bit_cast(float, __builtin_amdgcn_mov_dpp(__builtin_bit_cast(int, x), CTRL, 0xf, 0xf, true));
}
template <int CTRL> __device__ __forceinline__ u32 dppu(u32 x) {
    return (u32)__builtin_amdgcn_mov_dpp((int)x, CTRL, 0xf, 0xf, true);
}
constexpr int XOR1 = 0xB1, XOR2 = 0x4E, XOR7 = 0x141, ROR8 = 0x128, ROR4 = 0x124, BCAST = 0x150;
__device__ __forceinline__ float sw16_add(float a, float b) {
    auto s = __builtin_amdgcn_permlane16_swap(__float_as_uint(a), __float_as_uint(b), false, false);
    return __uint_as_float(s[0]) + __uint_as_float(s[1]);
}
__device__ __forceinline__ float sw32_add(float a, float b) {
    auto s = __builtin_amdgcn_permlane32_swap(__float_as_uint(a), __float_as_uint(b), false, false);
    return __uint_as_float(s[0]) + __uint_as_float(s[1]);
}
__device__ __forceinline__ float x16_max(float x) {
    auto s = __builtin_amdgcn_permlane16_swap(__float_as_uint(x), __float_as_uint(x), false, false);
    return fmaxf(__uint_as_float(s[0]), __uint_as_float(s[1]));
}
__device__ __forceinline__ float x32_max(float x) {
    auto s = __builtin_amdgcn_permlane32_swap(__float_as_uint(x), __float_as_uint(x), false, false);
    return fmaxf(__uint_as_float(s[0]), __uint_as_float(s[1]));
}
__device__ __forceinline__ float row16_sum(float x) {
    x += dpp<XOR1>(x); x += dpp<XOR2>(x); x += dpp<XOR7>(x); x += dpp<ROR8>(x); return x;
}
__device__ __forceinline__ float row16_max(float x) {
    x = fmaxf(x, dpp<XOR1>(x)); x = fmaxf(x, dpp<XOR2>(x)); x = fmaxf(x, dpp<XOR7>(x)); x = fmaxf(x, dpp<ROR8>(x)); return x;
}
__device__ __forceinline__ float wave_sum(float x) { x = row16_sum(x); x = sw16_add(x, x); return sw32_add(x, x); }
__device__ __forceinline__ f32x4 mfma(u32x4 a, u32x4 b, f32x4 c) {
    return __builtin_amdgcn_mfma_f32_16x16x32_bf16(__builtin_bit_cast(bf16x8, a), __builtin_bit_cast(bf16x8, b), c, 0, 0, 0);
}
typedef u32x4 rsrc_t;
__device__ u32x4 buf_ld4(u32x4 r, u32 voff, u32 soff, int aux) __asm("llvm.amdgcn.raw.buffer.load.v4i32");
__device__ u32 buf_ld1(u32x4 r, u32 voff, u32 soff, int aux) __asm("llvm.amdgcn.raw.buffer.load.i32");
__device__ void buf_st4(u32x4 v, u32x4 r, u32 voff, u32 soff, int aux) __asm("llvm.amdgcn.raw.buffer.store.v4i32");
__device__ __forceinline__ u32x4 ldnt(rsrc_t r, u32 voff, u32 soff) { return buf_ld4(r, voff, soff, AUX_NT); }
__device__ __forceinline__ u32x4 ldsc(rsrc_t r, u32 voff) { return buf_ld4(r, voff, 0, AUX_SC1); }
__device__ __forceinline__ void stsc(u32x4 v, rsrc_t r, u32 voff) { buf_st4(v, r, voff, 0, AUX_SC1); }
__device__ __forceinline__ bool tagok(u32x4 v, u32 tag) { return (v[1] == tag) & (v[3] == tag); }

#define CBAR() asm volatile("" ::: "memory")
#define LDS_FENCE() asm volatile("s_waitcnt lgkmcnt(0)" ::: "memory")
#define RAWBAR() do { CBAR(); __builtin_amdgcn_s_barrier(); CBAR(); } while (0)
#define BARRIER() do { LDS_FENCE(); __builtin_amdgcn_s_barrier(); CBAR(); } while (0)

#define TS(i) do { } while (0)
#define CNT() do { } while (0)
#define CNT2() do { } while (0)
#define CNT3() do { } while (0)
#define WRITE_STAMPS() do { } while (0)

enum { F_G = 0, F_PUB, F_K, F_Q, F_P0, F_P1, F_H, F_A, F_W, F_N };
struct Smem {
    u32 q[256];
    u32 p[2][8][4];
    float gp[4][64];
    float mx[2][2];
    float w[32];
    u32 a[8];
    float fin[4][8];
    u32 flag[16];
};
typedef __attribute__((address_space(3))) u32 lds_u32;
__device__ __forceinline__ void lds_wait(u32* f, u32 want) {
    volatile lds_u32* p = (volatile lds_u32*)f;
#pragma nounroll
    for (u32 i = 0; i < (1u << 22); ++i) { if (__builtin_amdgcn_readfirstlane(*p) >= want) break; }
    CBAR();
}
__device__ __forceinline__ void lds_wait_sleep(u32* f, u32 want) {
    volatile lds_u32* p = (volatile lds_u32*)f;
#pragma nounroll
    for (u32 i = 0; i < (1u << 22); ++i) { if (__builtin_amdgcn_readfirstlane(*p) >= want) break; __builtin_amdgcn_s_sleep(1); }
    CBAR();
}
__device__ __forceinline__ u32 lds_wait_val(u32* f, u32 have, u32 want) {
    volatile lds_u32* p = (volatile lds_u32*)f;
#pragma nounroll
    for (u32 i = 0; i < (1u << 22) && have < want; ++i) have = __builtin_amdgcn_readfirstlane(*p);
    CBAR();
    return have;
}
__device__ __forceinline__ void lds_inc(u32* f, int lane, u32 n = 1) {
    if (lane == 0) asm volatile("ds_add_u32 %0, %1" :: "v"((u32)(uintptr_t)(lds_u32*)f), "v"(n) : "memory");
}
__device__ __forceinline__ void lds_set(u32* f) {
    LDS_FENCE();
    *(volatile lds_u32*)f = 1u;
    CBAR();
}
__device__ __forceinline__ u32 now() { return (u32)__builtin_amdgcn_s_memrealtime(); }
__device__ __forceinline__ void time_wait(u32 t0, u32 dt) {
#pragma nounroll
    for (u32 i = 0; i < 4096; ++i) { if (now() - t0 >= dt) break; __builtin_amdgcn_s_sleep(1); }
    CBAR();
}
__device__ __forceinline__ u32x4 mask4(u32 v, const u32 (&mk)[4]) { return u32x4{v & mk[0], v & mk[1], v & mk[2], v & mk[3]}; }
__device__ __forceinline__ u32x4 perm4(u32 v, const u32 (&sl)[4]) {
    return u32x4{__builtin_amdgcn_perm(v, v, sl[0]), __builtin_amdgcn_perm(v, v, sl[1]), __builtin_amdgcn_perm(v, v, sl[2]), __builtin_amdgcn_perm(v, v, sl[3])};
}
__device__ __forceinline__ float rdl(float v, int l) { return __uint_as_float(__builtin_amdgcn_readlane(__float_as_uint(v), l)); }
__device__ __forceinline__ u32 tagbad(u32 acc, u32x4 v, u32 tag) { return (acc | (v[1] ^ tag)) | (v[3] ^ tag); }
__device__ __forceinline__ float fl(u32 v) { return __uint_as_float(v); }
constexpr u32 OOB = 0x80000000u;
#define WAITV(n) asm volatile("s_waitcnt vmcnt(%0)" :: "n"(n) : "memory")

struct Desc { const void* p; u32 n, f; };
struct Role { Desc a, b; u32 so[8]; };
struct Args { Role r[3]; Desc ws, wo, w2, v; const u16 *b1, *b2, *x; u16* out; };
static_assert(sizeof(Args) == 288, "no padding");
template <class T> __device__ __forceinline__ T* mkptr(u32 lo, u32 hi) { return (T*)(((uintptr_t)hi << 32) | lo); }
typedef const __attribute__((address_space(1))) u16 gu16;

extern "C" __global__ void __launch_bounds__(NT, 2)
fused_decode(const Args args) {
    __shared__ __attribute__((aligned(16))) Smem sm;
    const int tid = threadIdx.x, lane = tid & 63;
    const int wave = __builtin_amdgcn_readfirstlane(tid >> 6);
    typedef const volatile __attribute__((address_space(4))) char* kptr;
    const kptr kp = (kptr)__builtin_amdgcn_kernarg_segment_ptr();
    const u32x16 ka = *(const volatile __attribute__((address_space(4))) u32x16*)(kp + (wave >> 1) * 64);
    const u32x16 kb = *(const volatile __attribute__((address_space(4))) u32x16*)(kp + 0xc0);
    const u32x8 kc = *(const volatile __attribute__((address_space(4))) u32x8*)(kp + 0x100);
    const unsigned long long t64 = __builtin_amdgcn_s_memrealtime();
    const u32 t0 = (u32)t64;
    __builtin_amdgcn_sched_barrier(0);
    const int b = blockIdx.x, h = b & 7, s = b >> 3;
    u32 c0;
    {
        u32x2 pc; u32 pcl;
        asm volatile("s_getpc_b64 %0\n.Lpf%=:\n s_cmp_eq_u32 %2, 6\n s_cselect_b32 %1, fused_decode_end-.Lpf%=, 0" : "=&s"(pc), "=&s"(pcl) : "s"(wave) : "scc");
        c0 = buf_ld1(u32x4{pc[0], pc[1] & 0xffffu, pcl & 0xffffu, 0x00020000u}, (lane * PFSPLIT + (s & (PFSPLIT - 1))) * 128, 0, 0);
        __builtin_amdgcn_sched_barrier(0);
    }
    const int r16 = lane & 15, g = lane >> 4;
#define KARG_USE(o1, o2) \
        asm volatile("" :: "v"(o1), "v"(o2)); __builtin_amdgcn_sched_barrier(0); \
        asm volatile("" :: "s"(ka), "s"(kb), "s"(kc), "s"(t64)); __builtin_amdgcn_sched_barrier(0)
    const rsrc_t ra = {ka[0], ka[1], ka[2], ka[3]}, rb = {ka[4], ka[5], ka[6], ka[7]};
    const rsrc_t wsr = {kb[0], kb[1], kb[2], kb[3]};
    unsigned char* const ws = mkptr<unsigned char>(kb[0], kb[1]);
#define EPOCH_LOAD() const u32 epoch = *(const volatile __attribute__((address_space(4))) u32*)(uintptr_t)(ws + WS_EPOCH)
#define MK_TAG() __builtin_amdgcn_sched_barrier(0); ep1 = epoch + 1u; tag = ep1 * 0x9E3779B1u ^ 0x5BD1E995u; asm volatile("" :: "s"(tag))
    const u32 kvb = (u32)(h * SK + (SK - WIN) + s * 64) * (DH * 2);
    const u32 mi = h * 28 + s;
#define UBASE ((mi >> 3) * 64 + (mi & 4) * 8 + (mi & 3) * 2)
    u32 ep1, tag;
#define ROTOFF(a) (((((a) + ROTA) & 7)) * 2048)
#define H2ADDR(cons, prod) (WS_H2 + ((h * 16 + ((cons) >> 1)) * 32 + (prod)) * 128 + ((cons) & 1) * 64)
    constexpr u32 H2STEP = 16 * 128;
#define H2OFF(dh) (H2ADDR((g >> 1) * 16 + (dh) * 8 + (r16 >> 2) * 2 + (g & 1), s) + (r16 & 3) * 16)
#define H3OFF(T) ((16 * (T) + r16 < 56) ? WS_H3 + ((16 * (T) + (r16 & 12) + g) * 256 + h * 32 + s) * 64 + (r16 & 3) * 16 : OOB)

    if (wave >= 3) {
    if ((wave >> 1) != 2) {
        const int j = wave & 1;
        if (wave == 6) sm.flag[lane & 15] = 0u;
        const u32 qoff = WS_QG + h * 16384 + j * 2048 + lane * 16;
        KARG_USE(qoff, qoff);
        LDS_FENCE();
        EPOCH_LOAD();
        const u32 b1v = mkptr<gu16>(kc[0], kc[1])[(s < 28 ? UBASE : 0) + 8 * (lane & 3) + (lane >> 5)];
        RAWBAR();
        TS(1);
        MK_TAG();
        asm volatile("" :: "v"(c0), "v"(b1v));
        {
            u32x4 qa[8];
            lds_wait_sleep(&sm.flag[F_PUB], 1);
            __builtin_amdgcn_s_sleep(QDELAY);
            TS(2);
            u32 bad = 1, q0, q1;
#pragma nounroll
            for (u32 spin = 0; spin < SPIN_CAP; ++spin) {
                CBAR();
                CNT();
                if (bad) {
#pragma unroll
                    for (int i = 0; i < 8; ++i) qa[i] = buf_ld4(wsr, qoff + (i & 1) * 1024, (i >> 1) * 4096, AUX_SC1);
                    bad = 0;
#pragma unroll
                    for (int i = 0; i < 8; ++i) bad = tagbad(bad, qa[i], tag);
#define QSUM(j, e) ((fl(qa[j][e]) + fl(qa[2 + j][e])) + (fl(qa[4 + j][e]) + fl(qa[6 + j][e])))
                    q0 = cvt2(QSUM(0, 0), QSUM(0, 2)); q1 = cvt2(QSUM(1, 0), QSUM(1, 2));
                }
                if (__all(bad == 0)) break;
            }
            sm.q[128 * j + lane] = q0; sm.q[128 * j + 64 + lane] = q1;
            LDS_FENCE();
            lds_inc(&sm.flag[F_Q], lane);
            TS(3);
        }
        if (wave == 6) {
            u32 pp = 0;
            if (s < 28) {
                u32x4 ua;
#pragma nounroll
                for (u32 spin = 0; spin < SPIN_CAP; ++spin) {
                    CBAR();
                    CNT2();
                    ua = ldsc(wsr, WS_U + mi * 1024 + lane * 16);
                    if (__all(tagok(ua, tag))) break;
                    __builtin_amdgcn_s_sleep(RETRY);
                }
                float r = sw32_add(fl(ua[0]), fl(ua[2]));
                r = sw16_add(r, r);
                r += dpp<ROR8>(r); r += dpp<ROR4>(r);
                const float u = rbf(rbf(r) + bflo(b1v));
                const float zz = 0.7978845608028654f * (u + 0.044715f * u * u * u);
                const float th = 1.f - 2.f * __builtin_amdgcn_rcpf(__builtin_amdgcn_exp2f(2.f * LOG2E * zz) + 1.f);
                const float hv = 0.5f * u * (1.f + th);
                pp = cvt2(hv, hv);
            }
            if ((lane & 28) == 0) sm.a[2 * (lane & 3) + (lane >> 5)] = pp;
            TS(4);
            lds_wait_sleep(&sm.flag[F_P1], 1);
            time_wait(t0, ST);
            TS(5);
            u32x4 sa;
#pragma nounroll
            for (u32 spin = 0; spin < SPIN_CAP; ++spin) {
                CBAR();
                CNT3();
                sa = ldsc(wsr, WS_ST + ((h * 2 + (s >> 4)) * 64 + lane) * 16);
                if (__all(tagok(sa, tag))) break;
                __builtin_amdgcn_s_sleep(RETRY);
            }
            TS(6);
            const float M = x32_max(x16_max(row16_max(fl(sa[0]))));
            const float e = __builtin_amdgcn_exp2f((fl(sa[0]) - M) * LOG2E);
            const float L = wave_sum(e * fl(sa[2]));
            const float w = fmaxf(e, dpp<XOR1>(e)) * __builtin_amdgcn_rcpf(L);
            if ((lane & 1) == 0) sm.w[(lane & 30) + (lane >> 5)] = w;
            lds_set(&sm.flag[F_A]);
            TS(7);
            WRITE_STAMPS();
            __builtin_amdgcn_endpgm();
        }
    } else {
        const int kw = wave - 4;
        const u32 koff = kvb + kw * (32 * DH * 2) + (r16 >> 2) * 512 + (4 * g + (r16 & 3)) * 16;
        KARG_USE(koff, koff);
        EPOCH_LOAD();
        RAWBAR();
        u32 so[8];
#pragma unroll
        for (int a = 0; a < 8; ++a) { so[a] = ROTOFF(a); asm volatile("" : "+s"(so[a])); }
        time_wait(t0, KT);
        TS(1);
        u32x4 kf[16];
#pragma unroll
        for (int i = 0; i < 16; ++i) {
            kf[i] = ldnt(ra, koff + (i & 1) * 256, so[i >> 1]);
            if (i < 15) PACE(i, KP);
        }
        TS(2);
        MK_TAG();
        asm volatile("" :: "v"(c0));
        TS(3);
        lds_wait(&sm.flag[F_Q], 2);
        TS(4);
        const u32* qp = sm.q + ((r16 >> 2) & 1) * 128 + (4 * g + (r16 & 3)) * 4;
        const u32x4 q0 = *reinterpret_cast<const u32x4*>(qp), q1 = *reinterpret_cast<const u32x4*>(qp + 64);
        float t[8];
#pragma unroll
        for (int a = 0; a < 8; ++a) {
            f32x4 d = {0.f, 0.f, 0.f, 0.f};
            d = mfma(q0, kf[2 * a], d);
            d = mfma(q1, kf[2 * a + 1], d);
            float v = d[a & 3];
            if ((a & 3) != 0) v += dpp<0x00>(d[0]);
            if ((a & 3) != 1) v += dpp<0x55>(d[1]);
            if ((a & 3) != 2) v += dpp<0xaa>(d[2]);
            if ((a & 3) != 3) v += dpp<0xff>(d[3]);
            t[a] = v;
        }
        const bool hi = lane >= 32;
        const float x0 = hi ? t[4] : t[0], x1 = hi ? t[5] : t[1], x2 = hi ? t[6] : t[2], x3 = hi ? t[7] : t[3];
        const float y0 = (lane & 1) ? x1 : x0, y1 = (lane & 1) ? x3 : x2;
        const float z = rbf((lane & 2) ? y1 : y0) * 0.0625f;
        const float m = x32_max(row16_max(z));
        const float e = __builtin_amdgcn_exp2f((z - m) * LOG2E);
        reinterpret_cast<u16*>(sm.p[kw][(r16 >> 2) * 2 + (g & 1)])[4 * (g >> 1) + (lane & 3)] = (u16)cvt2(e, e);
        sm.mx[g & 1][kw ^ ROTH] = m;
        lds_set(&sm.flag[F_P0 + kw]);
        float l = row16_sum(e);
        l = sw32_add(l, l);
        if ((lane & 47) == 0) stsc(u32x4{__float_as_uint(m), tag, __float_as_uint(l), tag}, wsr, WS_ST + ((h * 2 + g) * 64 + 2 * s + kw) * 16);
        TS(5);
    }
    } else {
        const bool isw1 = wave == 2;
        u32 rg, rstr, gcol;
        asm("s_cmp_eq_u32 %3, 2\n s_cselect_b32 %0, %4, %5\n s_cselect_b32 %1, %6, %7\n s_cselect_b32 %2, %8, %9" : "=&s"(rg), "=&s"(rstr), "=&s"(gcol)
            : "s"(wave), "s"(mi & 7), "s"((s >> 3) * 2 + wave), "s"(F * 16), "s"(NQ * 16), "s"(mi >> 3), "s"(h * 8 + (s & 7)) : "scc");
        rg *= 7;
        const u32 glim = wave < (s < 28 ? 3 : 2) ? 56 : 0;
        const bool gon = glim != 0;
        u32 goff = __umul24(rg + (lane >> 3), rstr) + (gcol * 8 + (lane & 7)) * 16;
        goff = lane < glim ? goff : OOB;
        const u32 xoff = lane * 16;
        KARG_USE(goff, xoff);
        const u32x4 xv = buf_ld4(rb, xoff, 0, 0);
        if (isw1) { RAWBAR(); time_wait(t0, W1T); }
        u32x4 wf[8];
        wf[0] = ldnt(ra, goff, 0);
#pragma unroll
        for (int i = 1; i < 8; ++i) wf[i] = ldnt(ra, goff, ka[7 + i]);
        EPOCH_LOAD();
        u32 sel[2][4];
#pragma unroll
        for (int d = 0; d < 4; ++d) {
            const bool on = ((lane & 7) >> 1) == d;
            sel[0][d] = on ? ((lane & 1) ? 0x01000c0cu : 0x0c0c0100u) : 0x0c0c0c0cu;
            sel[1][d] = on ? ((lane & 1) ? 0x03020c0cu : 0x0c0c0302u) : 0x0c0c0c0cu;
            asm volatile("" : "+v"(sel[0][d]), "+v"(sel[1][d]));
        }
        if (!isw1) RAWBAR();
        TS(1);
        u32 xp[4] = {xv[0], xv[1], xv[2], xv[3]};
#pragma nounroll
        for (int it = isw1 ? 0 : 1; it < 2; ++it) {
            float v[8];
#pragma unroll
            for (int d = 0; d < 4; ++d) { v[2 * d] = bflo(xp[d]); v[2 * d + 1] = bfhi(xp[d]); }
            const float c = rdl(v[0], 0);
            float s1 = 0.f, s2 = 0.f;
#pragma unroll
            for (int j = 0; j < 8; ++j) { const float d = v[j] - c; s1 += d; s2 = fmaf(d, d, s2); }
            if (lane >= 56) { s1 = 0.f; s2 = 0.f; }
            float r = row16_sum(sw32_add(s1, s2));
            r = sw16_add(r, r) * (1.f / D);
            const float md = rdl(r, 0), var = fmaxf(fmaf(-md, md, rdl(r, 32)), 0.f);
            const float mb = rbf(c + md);
            const float rden = __builtin_amdgcn_rcpf(rbf(__builtin_amdgcn_sqrtf(rbf(rbf(var) + 1e-5f))));
#pragma unroll
            for (int d = 0; d < 4; ++d) {
                const u32 t = cvt2(v[2 * d] - mb, v[2 * d + 1] - mb);
                xp[d] = cvt2(bflo(t) * rden, bfhi(t) * rden);
            }
        }
        u32 yp[4];
#pragma unroll
        for (int d = 0; d < 4; ++d) yp[d] = __builtin_amdgcn_ds_bpermute((rg + (lane >> 3)) * 4, xp[d]);
        MK_TAG();
        TS(2);
        f32x4 acc = {0.f, 0.f, 0.f, 0.f};
#pragma unroll
        for (int i = 0; i < 8; ++i) acc = mfma(perm4(yp[i >> 1], sel[i & 1]), wf[i], acc);
        asm volatile("" :: "v"(c0));
        const bool vl = ((lane >> 3) & 1) == (lane >> 5);
        if (wave < 2) {
            if (vl) *reinterpret_cast<f32x4*>(&sm.gp[2 * wave + (g >> 1)][8 * (lane & 7) + 4 * (g & 1)]) = acc;
            LDS_FENCE();
            u32 old = 0;
            if (lane == 0) old = __hip_atomic_fetch_add((lds_u32*)&sm.flag[F_G], 1u, __ATOMIC_RELAXED, __HIP_MEMORY_SCOPE_WORKGROUP);
            if (__builtin_amdgcn_readfirstlane(old) == 1) {
                CBAR();
                const int ci = 2 * (lane & 31);
                const f32x2 r = (*reinterpret_cast<const f32x2*>(&sm.gp[0][ci]) + *reinterpret_cast<const f32x2*>(&sm.gp[1][ci])) +
                                (*reinterpret_cast<const f32x2*>(&sm.gp[2][ci]) + *reinterpret_cast<const f32x2*>(&sm.gp[3][ci]));
                if (lane < 32) stsc(u32x4{__float_as_uint(r[0]), tag, __float_as_uint(r[1]), tag}, wsr, WS_QG + (h * 32 + s) * 512 + lane * 16);
                CBAR();
                *(volatile lds_u32*)&sm.flag[F_PUB] = 1u;
            }
            TS(3);
            const rsrc_t vr = {kb[12], kb[13], kb[14], kb[15]};
            const int rh = ROTH;
            const u32 voff = kvb + rh * 16384 + wave * 256 + g * 512 + r16 * 16, voff1 = voff ^ 16384;
            u32 so[8];
#pragma unroll
            for (int a = 0; a < 8; ++a) { so[a] = ROTOFF(a); asm volatile("" : "+s"(so[a])); }
            const u32 hoff = H2OFF(wave);
            asm volatile("" :: "v"(voff), "v"(voff1), "v"(hoff));
            u32x4 vf[16];
            time_wait(t0, VT);
            TS(4);
#pragma unroll
            for (int t = 0; t < 16; ++t) {
                vf[t] = ldnt(vr, t < 8 ? voff : voff1, so[t & 7]);
                if (t < 15) PACE(t, VP);
            }
            TS(5);
            u32x4 af[16];
#pragma unroll
            for (int kh = 0; kh < 2; ++kh) {
                lds_wait(&sm.flag[F_P0 + (kh ^ rh)], 1);
                const u32x4 pv = *reinterpret_cast<const u32x4*>(sm.p[kh ^ rh][lane >> 3]);
#pragma unroll
                for (int t = 0; t < 8; ++t) { af[8 * kh + t] = perm4(pv[t >> 1], sel[t & 1]); asm volatile("" : "+v"(af[8 * kh + t])); }
            }
            const f32x2 mm = *reinterpret_cast<const f32x2*>(sm.mx[g >> 1]);
            const float mw = fmaxf(mm[0], mm[1]);
            const float al0 = __builtin_amdgcn_exp2f((mm[0] - mw) * LOG2E), al1 = __builtin_amdgcn_exp2f((mm[1] - mw) * LOG2E);
            TS(6);
            f32x4 o = {0.f, 0.f, 0.f, 0.f}, o1 = o;
#pragma unroll
            for (int t = 0; t < 8; ++t) o = mfma(af[t], vf[t], o);
            o *= al0;
            asm volatile("" : "+v"(o));
#pragma unroll
            for (int t = 8; t < 16; ++t) o1 = mfma(af[t], vf[t], o1);
            o += al1 * o1;
            stsc(u32x4{cvt2(o[0], o[1]), tag, cvt2(o[2], o[3]), tag}, wsr, hoff);
            CBAR();
            lds_inc(&sm.flag[F_H], lane);
            TS(7);
            WRITE_STAMPS();
        } else if (gon) {
            const float p2 = dpp<ROR8>(acc[2]), p3 = dpp<ROR8>(acc[3]);
            const float u0 = vl ? acc[0] : p2, u1 = vl ? acc[1] : p3;
            stsc(u32x4{__float_as_uint(u0), tag, __float_as_uint(u1), tag}, wsr,
                 WS_U + (((mi & ~7u) + (lane & 4) + (g & 1) * 2 + (vl ? 0 : 1)) * 16 + (mi & 7) * 2 + (g >> 1)) * 64 + (lane & 3) * 16);
            TS(3);
        }
    }
    if (wave >= 2) {
        const int T = wave - 2;
        const rsrc_t wor = {kb[4], kb[5], kb[6], kb[7]}, w2r = {kb[8], kb[9], kb[10], kb[11]};
        const u32 coff = (16 * T + r16 < 56) ? (u32)(128 * T + 8 * r16) * 2 : OOB;
        const u32 o2 = s < 28 ? coff + (UBASE + 8 * g) * (D * 2) : OOB, oo = coff + (h * 512 + (s >> 1) * 32 + (s & 1) * 4 + g) * (D * 2);
        const u32 doff = H2ADDR(s, lane >> 2) + (lane & 3) * 16;
        const u32 h3off = H3OFF(T);
        u32 mk[4];
#pragma unroll
        for (int d = 0; d < 4; ++d) mk[d] = (((lane & 7) >> 1) == d) ? ((lane & 1) ? 0xffff0000u : 0x0000ffffu) : 0u;
        asm volatile("" :: "v"(o2), "v"(oo), "v"(doff), "v"(h3off), "v"(mk[0]), "v"(mk[1]), "v"(mk[2]), "v"(mk[3]));
        time_wait(t0, TAILT);
        TS(6);
        u32x4 tf[6];
        tf[4] = ldnt(w2r, o2, 0); PACE(0, TP); tf[5] = ldnt(w2r, o2 + D * 2, 0);
#pragma unroll
        for (int t = 0; t < 4; ++t) { PACE(t + 1, TP); tf[t] = ldnt(wor, oo, t * 8 * D * 2); }
        lds_wait_sleep(&sm.flag[F_A], 1);
        const u32x2 ah = *reinterpret_cast<const u32x2*>(&sm.a[2 * g]);
        const f32x2 wv = *reinterpret_cast<const f32x2*>(&sm.w[2 * (lane >> 2)]);
        f32x4 acc = {0.f, 0.f, 0.f, 0.f};
        acc = mfma(mask4(ah[0], mk), tf[4], acc); acc = mfma(mask4(ah[1], mk), tf[5], acc);
        asm volatile("" : "+v"(acc) : "v"(wv));
        TS(7);
        lds_wait_sleep(&sm.flag[F_H], 2);
        if (H2DELAY) __builtin_amdgcn_s_sleep(H2DELAY);
        if (H2T) time_wait(t0, H2T);
        TS(8);
        float v0, v1, v2, v3;
        {
            u32 bad = 1;
#pragma nounroll
            for (u32 spin = 0; spin < SPIN_CAP; ++spin) {
                CBAR();
                CNT2();
                if (bad) {
                    const u32x4 da = ldsc(wsr, doff), db = ldsc(wsr, doff + H2STEP);
                    bad = tagbad(tagbad(0, da, tag), db, tag);
                    v0 = fmaf(wv[1], bflo(db[0]), wv[0] * bflo(da[0])); v1 = fmaf(wv[1], bfhi(db[0]), wv[0] * bfhi(da[0]));
                    v2 = fmaf(wv[1], bflo(db[2]), wv[0] * bflo(da[2])); v3 = fmaf(wv[1], bfhi(db[2]), wv[0] * bfhi(da[2]));
                }
                if (__all(bad == 0)) break;
                __builtin_amdgcn_s_sleep(RETRY);
            }
        }
        TS(9);
        float v = sw16_add(sw32_add(v0, v2), sw32_add(v1, v3));
        v += dpp<ROR8>(v); v += dpp<ROR4>(v);
        const u32 ov = cvt2(v, v);
        acc = mfma(mask4(dppu<0x00>(ov), mk), tf[0], acc);
        acc = mfma(mask4(dppu<0x55>(ov), mk), tf[1], acc);
        acc = mfma(mask4(dppu<0xaa>(ov), mk), tf[2], acc);
        acc = mfma(mask4(dppu<0xff>(ov), mk), tf[3], acc);
        {
            const float c0 = g < 2 ? acc[0] : acc[2], c1 = g < 2 ? acc[1] : acc[3];
            stsc(u32x4{__float_as_uint(c0), tag, __float_as_uint(c1), tag}, wsr, h3off);
        }
        TS(10);
        if (s >= 25) {
            const int cg = (s - 25) * 8 + h;
            const u32 col = (cg >> 2) * 32 + (cg & 1) * 4 + (cg & 2) + 8 * ((lane >> 1) & 3) + (lane & 1);
            const u32 xr = mkptr<gu16>(kc[4], kc[5])[col], b2r = mkptr<gu16>(kc[2], kc[3])[col];
            const u32 off = WS_H3 + (cg * 256 + T * 64) * 64 + lane * 16;
            __builtin_amdgcn_s_sleep(H3DELAY);
            float c0, c1;
            u32 bad = 1;
#pragma nounroll
            for (u32 spin = 0; spin < SPIN_CAP; ++spin) {
                CBAR();
                CNT3();
                if (bad) {
                    const u32x4 ga = ldsc(wsr, off), gb = ldsc(wsr, off + 1024), gc = ldsc(wsr, off + 2048), gd = ldsc(wsr, off + 3072);
                    bad = tagbad(tagbad(tagbad(tagbad(0, ga, tag), gb, tag), gc, tag), gd, tag);
                    c0 = (fl(ga[0]) + fl(gb[0])) + (fl(gc[0]) + fl(gd[0]));
                    c1 = (fl(ga[2]) + fl(gb[2])) + (fl(gc[2]) + fl(gd[2]));
                }
                if (__all(bad == 0)) break;
                __builtin_amdgcn_s_sleep(RETRY);
            }
            float c = sw32_add(c0, c1);
            c = sw16_add(c, c);
            c += dpp<ROR8>(c); c += dpp<ROR4>(c);
            if ((lane & 28) == 0) sm.fin[T][2 * (lane & 3) + (lane >> 5)] = c;
            BARRIER();
            if (wave == 2 && lane < 8) {
                const float r = (bflo(xr) + bflo(b2r)) + ((sm.fin[0][lane] + sm.fin[1][lane]) + (sm.fin[2][lane] + sm.fin[3][lane]));
                mkptr<__attribute__((address_space(1))) u16>(kc[6], kc[7])[col] = (u16)(cvt2(r, r) >> 16);
                if (cg == 0 && lane == 0)
                    __hip_atomic_store(mkptr<__attribute__((address_space(1))) u32>(kb[0], kb[1]) + WS_EPOCH / 4, ep1, __ATOMIC_RELAXED, __HIP_MEMORY_SCOPE_AGENT);
            }
        }
        WRITE_STAMPS();
    }
}

extern "C" __device__ __attribute__((used, noinline)) void fused_decode_end() {}

extern "C" void kernel_launch(void* const* d_in, const int* in_sizes, int n_in,
                              void* d_out, int out_size, void* d_ws, size_t ws_size,
                              hipStream_t stream) {
    const u32 FL = 0x00020000u, BIG = 0x7fffffffu;
    Args a;
    const void* pa[3] = {d_in[3], d_in[6], d_in[1]};
    const u32 st[3] = {NQ * 2, F * 2, 2048};
    for (int r = 0; r < 3; ++r) {
        a.r[r].a = Desc{pa[r], BIG, FL};
        a.r[r].b = Desc{d_in[0], (u32)(D * 2), FL};
        for (int i = 0; i < 8; ++i) a.r[r].so[i] = (i + 1) * st[r];
    }
    a.ws = Desc{d_ws, WS_END, FL};
    a.wo = Desc{d_in[4], BIG, FL};
    a.w2 = Desc{d_in[8], BIG, FL};
    a.v = Desc{d_in[2], BIG, FL};
    a.b1 = (const u16*)d_in[7]; a.b2 = (const u16*)d_in[9]; a.x = (const u16*)d_in[0]; a.out = (u16*)d_out;
    fused_decode<<<NWG, NT, 0, stream>>>(a);
}
```

```cpp
#include <hip/hip_runtime.h>
#include <hip/hip_bf16.h>
#include <stdint.h>

typedef unsigned short u16;
typedef unsigned u32;
typedef __attribute__((ext_vector_type(4))) unsigned u32x4;
typedef __attribute__((ext_vector_type(2))) unsigned u32x2;
typedef __attribute__((ext_vector_type(8))) unsigned u32x8;
typedef __attribute__((ext_vector_type(16))) unsigned u32x16;
typedef __attribute__((ext_vector_type(4))) float f32x4;
typedef __attribute__((ext_vector_type(2))) float f32x2;
typedef __attribute__((ext_vector_type(8))) __bf16 bf16x8;
typedef __attribute__((ext_vector_type(2))) __bf16 bf16x2;

constexpr int D = 448, SK = 8192, DH = 256, WIN = 2048, F = 1792, NQ = 4096;
constexpr int NWG = 256, NT = 512;
constexpr u32 WS_EPOCH = 0;
constexpr u32 WS_ST = 4096;
constexpr u32 WS_QG = 4096 + 65536;
constexpr u32 WS_U = WS_QG + 8 * 16384;
constexpr u32 WS_H2 = WS_U + 224 * 1024;
constexpr u32 WS_H3 = WS_H2 + 8 * 32 * 32 * 64;
constexpr u32 WS_END = WS_H3 + 56 * 256 * 64;
constexpr u32 SPIN_CAP = 1u << 16;
constexpr float LOG2E = 1.4426950408889634f;
#define AUXL 2
constexpr int AUX_NT = AUXL, AUX_SC1 = 16;

#define W1T 85
#define KT 95
#define KP 210
#define VT 235
#define VP 210
#define TAILT 375
#define TP 300
#define ST 470
#define QDELAY 8
#define EN 165
#define SA 4
#define SC SA
#define TLBP 1
#define TLBK 1
#define QT2 140
#define SB 4
#define STH 2
#define HC 30
#define DCAP 100
#define ROTA (s >> 2)
#define ROTH (h & 1)
#define KV_WG ((s >> 2) * 131072 + (s & 3) * 8192)
#define KV_HALF 4096
#define KV_SUB 32768
#define KV_BLK 512
#define SUBOFF(k) ((k) * KV_SUB)
#define PACE(i, P) do { if (((i) + 1) * (P) / 64 - (i) * (P) / 64 > (P) / 64) __builtin_amdgcn_s_sleep((P) / 64 + 1); else if ((P) >= 64) __builtin_amdgcn_s_sleep((P) / 64); CBAR(); } while (0)
#define H2T 520
#define H3DELAY 14
#define RETRY 1
#define PFSPLIT 4

__device__ __forceinline__ float fmx(float a, float b) {
    float r;
    asm("v_max_f32 %0, %1, %2" : "=v"(r) : "v"(a), "v"(b));
    return r;
}
#define MAXDPP(ctl) "s_nop 1\n v_max_f32_dpp %0, %0, %0 " ctl " row_mask:0xf bank_mask:0xf\n"
__device__ __forceinline__ u32 cvt2(float lo, float hi) {
    const f32x2 v = {lo, hi};
    return __builtin_bit_cast(u32, __builtin_convertvector(v, bf16x2));
}
__device__ __forceinline__ float bflo(u32 v) { return __uint_as_float(v << 16); }
__device__ __forceinline__ float bfhi(u32 v) { return __uint_as_float(v & 0xffff0000u); }
__device__ __forceinline__ float rbf(float f) { return bfhi(cvt2(f, f)); }
__device__ __forceinline__ float dot2(u32 a, u32 b, float c) {
    return __builtin_amdgcn_fdot2_f32_bf16(__builtin_bit_cast(bf16x2, a), __builtin_bit_cast(bf16x2, b), c, false);
}
template <int CTRL> __device__ __forceinline__ float dpp(float x) {
    return __builtin_bit_cast(float, __builtin_amdgcn_mov_dpp(__builtin_bit_cast(int, x), CTRL, 0xf, 0xf, true));
}
template <int CTRL> __device__ __forceinline__ u32 dppu(u32 x) {
    return (u32)__builtin_amdgcn_mov_dpp((int)x, CTRL, 0xf, 0xf, true);
}
constexpr int XOR1 = 0xB1, XOR2 = 0x4E, XOR7 = 0x141, ROR8 = 0x128, ROR4 = 0x124, BCAST = 0x150;
__device__ __forceinline__ float sw16_add(float a, float b) {
    auto s = __builtin_amdgcn_permlane16_swap(__float_as_uint(a), __float_as_uint(b), false, false);
    return __uint_as_float(s[0]) + __uint_as_float(s[1]);
}
__device__ __forceinline__ float sw32_add(float a, float b) {
    auto s = __builtin_amdgcn_permlane32_swap(__float_as_uint(a), __float_as_uint(b), false, false);
    return __uint_as_float(s[0]) + __uint_as_float(s[1]);
}
__device__ __forceinline__ float x16_max(float x) {
    auto s = __builtin_amdgcn_permlane16_swap(__float_as_uint(x), __float_as_uint(x), false, false);
    return fmx(__uint_as_float(s[0]), __uint_as_float(s[1]));
}
__device__ __forceinline__ float x32_max(float x) {
    auto s = __builtin_amdgcn_permlane32_swap(__float_as_uint(x), __float_as_uint(x), false, false);
    return fmx(__uint_as_float(s[0]), __uint_as_float(s[1]));
}
__device__ __forceinline__ float row16_sum(float x) {
    x += dpp<XOR1>(x); x += dpp<XOR2>(x); x += dpp<XOR7>(x); x += dpp<ROR8>(x); return x;
}
__device__ __forceinline__ float row16_max(float x) {
    asm(MAXDPP("quad_perm:[1,0,3,2]") MAXDPP("quad_perm:[2,3,0,1]") MAXDPP("row_ror:4") MAXDPP("row_ror:8") : "+v"(x));
    return x;
}
__device__ __forceinline__ float pair_max(float e) {
    const u32 a = __float_as_uint(e), b = dppu<XOR1>(a);
    return __uint_as_float(a > b ? a : b);
}
__device__ __forceinline__ float wave_sum(float x) { x = row16_sum(x); x = sw16_add(x, x); return sw32_add(x, x); }
__device__ __forceinline__ f32x4 mfma(u32x4 a, u32x4 b, f32x4 c) {
    return __builtin_amdgcn_mfma_f32_16x16x32_bf16(__builtin_bit_cast(bf16x8, a), __builtin_bit_cast(bf16x8, b), c, 0, 0, 0);
}
typedef u32x4 rsrc_t;
__device__ u32x4 buf_ld4(u32x4 r, u32 voff, u32 soff, int aux) __asm("llvm.amdgcn.raw.buffer.load.v4i32");
__device__ u32 buf_ld1(u32x4 r, u32 voff, u32 soff, int aux) __asm("llvm.amdgcn.raw.buffer.load.i32");
__device__ void buf_st4(u32x4 v, u32x4 r, u32 voff, u32 soff, int aux) __asm("llvm.amdgcn.raw.buffer.store.v4i32");
__device__ __forceinline__ u32x4 ldnt(rsrc_t r, u32 voff, u32 soff) { return buf_ld4(r, voff, soff, AUX_NT); }
__device__ __forceinline__ u32x4 ldsc(rsrc_t r, u32 voff) { return buf_ld4(r, voff, 0, AUX_SC1); }
#define AUXQ 16
#define AUXS 16
#define AUXH 16
__device__ __forceinline__ void stsc(u32x4 v, rsrc_t r, u32 voff) { buf_st4(v, r, voff, 0, AUX_SC1); }
__device__ __forceinline__ bool tagok(u32x4 v, u32 tag) { return (v[1] == tag) & (v[3] == tag); }

#define CBAR() asm volatile("" ::: "memory")
#define LDS_FENCE() asm volatile("s_waitcnt lgkmcnt(0)" ::: "memory")
#define RAWBAR() do { CBAR(); __builtin_amdgcn_s_barrier(); CBAR(); } while (0)
#define BARRIER() do { LDS_FENCE(); __builtin_amdgcn_s_barrier(); CBAR(); } while (0)


enum { F_G = 0, F_PUB, F_U, F_Q, F_P0, F_P1, F_H, F_A, F_W, F_N };
struct Smem {
    u32 q[256];
    u32 p[2][8][4];
    float gp[4][64];
    float mx[2][2];
    float w[32];
    u32 a[8];
    float fin[4][16];
    u32 flag[16];
};
typedef __attribute__((address_space(3))) u32 lds_u32;
__device__ __forceinline__ void lds_wait(u32* f, u32 want) {
    volatile lds_u32* p = (volatile lds_u32*)f;
#pragma nounroll
    for (u32 i = 0; i < (1u << 22); ++i) { if (__builtin_amdgcn_readfirstlane(*p) >= want) break; }
    CBAR();
}
__device__ __forceinline__ void lds_wait_sleep(u32* f, u32 want) {
    volatile lds_u32* p = (volatile lds_u32*)f;
#pragma nounroll
    for (u32 i = 0; i < (1u << 22); ++i) { if (__builtin_amdgcn_readfirstlane(*p) >= want) break; __builtin_amdgcn_s_sleep(1); }
    CBAR();
}
__device__ __forceinline__ u32 lds_wait_val(u32* f, u32 have, u32 want) {
    volatile lds_u32* p = (volatile lds_u32*)f;
#pragma nounroll
    for (u32 i = 0; i < (1u << 22) && have < want; ++i) have = __builtin_amdgcn_readfirstlane(*p);
    CBAR();
    return have;
}
__device__ __forceinline__ void lds_inc(u32* f, int lane, u32 n = 1) {
    if (lane == 0) asm volatile("ds_add_u32 %0, %1" :: "v"((u32)(uintptr_t)(lds_u32*)f), "v"(n) : "memory");
}
__device__ __forceinline__ void lds_set(u32* f) {
    LDS_FENCE();
    *(volatile lds_u32*)f = 1u;
    CBAR();
}
__device__ __forceinline__ u32 now() { return (u32)__builtin_amdgcn_s_memrealtime(); }
__device__ __forceinline__ void time_wait(u32 t0, u32 dt) {
#pragma nounroll
    for (u32 i = 0; i < 4096; ++i) { if (now() - t0 >= dt) break; __builtin_amdgcn_s_sleep(1); }
    CBAR();
}
__device__ __forceinline__ u32x4 mask4(u32 v, const u32 (&mk)[4]) { return u32x4{v & mk[0], v & mk[1], v & mk[2], v & mk[3]}; }
__device__ __forceinline__ u32x4 perm4(u32 v, const u32 (&sl)[4]) {
    return u32x4{__builtin_amdgcn_perm(v, v, sl[0]), __builtin_amdgcn_perm(v, v, sl[1]), __builtin_amdgcn_perm(v, v, sl[2]), __builtin_amdgcn_perm(v, v, sl[3])};
}
__device__ __forceinline__ float rdl(float v, int l) { return __uint_as_float(__builtin_amdgcn_readlane(__float_as_uint(v), l)); }
__device__ __forceinline__ u32 tagbad(u32 acc, u32x4 v, u32 tag) { return (acc | (v[1] ^ tag)) | (v[3] ^ tag); }
__device__ __forceinline__ float fl(u32 v) { return __uint_as_float(v); }
constexpr u32 OOB = 0x80000000u;
#define WAITV(n) asm volatile("s_waitcnt vmcnt(%0)" :: "n"(n) : "memory")

struct Desc { const void* p; u32 n, f; };
struct Role { Desc a, b; u32 so[8]; };
struct Args { Role r[3]; Desc ws, wo, w2, v; const u16 *b1, *b2, *x; u16* out; };
static_assert(sizeof(Args) == 288, "no padding");
template <class T> __device__ __forceinline__ T* mkptr(u32 lo, u32 hi) { return (T*)(((uintptr_t)hi << 32) | lo); }
typedef const __attribute__((address_space(1))) u16 gu16;

extern "C" __global__ void __launch_bounds__(NT, 2)
fused_decode(const Args args) {
    __shared__ __attribute__((aligned(16))) Smem sm;
    const int tid = threadIdx.x, lane = tid & 63;
    const int wave = __builtin_amdgcn_readfirstlane(tid >> 6);
    typedef const volatile __attribute__((address_space(4))) char* kptr;
    const kptr kp = (kptr)__builtin_amdgcn_kernarg_segment_ptr();
    const u32x16 ka = *(const volatile __attribute__((address_space(4))) u32x16*)(kp + (wave >> 1) * 64);
    const u32x16 kb = *(const volatile __attribute__((address_space(4))) u32x16*)(kp + 0xc0);
    const u32x8 kc = *(const volatile __attribute__((address_space(4))) u32x8*)(kp + 0x100);
    const unsigned long long t64 = __builtin_amdgcn_s_memrealtime();
    const u32 t0 = (u32)t64;
    __builtin_amdgcn_sched_barrier(0);
    const int b = blockIdx.x, h = b & 7, s = b >> 3;
    u32 c0;
    {
        u32x2 pc; u32 pcl;
        asm volatile("s_getpc_b64 %0\n.Lpf%=:\n s_cmp_eq_u32 %2, 7\n s_cselect_b32 %1, fused_decode_end-.Lpf%=, 0" : "=&s"(pc), "=&s"(pcl) : "s"(wave) : "scc");
        c0 = buf_ld1(u32x4{pc[0], pc[1] & 0xffffu, pcl & 0xffffu, 0x00020000u}, (lane * PFSPLIT + (s & (PFSPLIT - 1))) * 128, 0, 0);
        __builtin_amdgcn_sched_barrier(0);
    }
    const int r16 = lane & 15, g = lane >> 4;
#define KARG_USE(o1, o2) \
        asm volatile("" :: "v"(o1), "v"(o2)); __builtin_amdgcn_sched_barrier(0); \
        asm volatile("" :: "s"(ka), "s"(kb), "s"(kc), "s"(t64)); __builtin_amdgcn_sched_barrier(0)
    const rsrc_t ra = {ka[0], ka[1], ka[2], ka[3]}, rb = {ka[4], ka[5], ka[6], ka[7]};
    const rsrc_t wsr = {kb[0], kb[1], kb[2], kb[3]};
    unsigned char* const ws = mkptr<unsigned char>(kb[0], kb[1]);
#define EPOCH_LOAD() const u32 epoch = *(const volatile __attribute__((address_space(4))) u32*)(uintptr_t)(ws + WS_EPOCH)
#define MK_TAG() __builtin_amdgcn_sched_barrier(0); ep1 = epoch + 1u; tag = ep1 * 0x9E3779B1u ^ 0x5BD1E995u; asm volatile("" :: "s"(tag))
    const u32 kvb = (u32)(h * SK + (SK - WIN)) * (DH * 2) + KV_WG;
    const u32 mi = h * 28 + s;
#define UBASE ((mi >> 3) * 64 + (mi & 4) * 8 + (mi & 3) * 2)
    u32 ep1, tag;
#define DSH(f) (((lds_wait_val(&sm.flag[F_PUB], 0, 1) - 1) * (f)) >> 2)
#define ROTOFF(a) (((((a) + ROTA) & 7)) * KV_BLK)
#define H2ADDR(cons, prod) (WS_H2 + ((h * 16 + ((cons) >> 1)) * 32 + (prod)) * 128 + ((cons) & 1) * 64)
    constexpr u32 H2STEP = 16 * 128;
#define H2OFF(dh) (H2ADDR((g >> 1) * 16 + (dh) * 8 + (r16 >> 2) * 2 + (g & 1), s) + (r16 & 3) * 16)
#define H3OFF(T) ((16 * (T) + r16 < 56) ? WS_H3 + ((8 * (T) + (r16 >> 2) * 2 + (g >> 1)) * 256 + h * 32 + s) * 128 + (g & 1) * 64 + (r16 & 3) * 16 : OOB)

    if (wave >= 3) {
    if ((wave >> 1) != 2) {
        const int j = wave & 1;
        if (wave == 7) sm.flag[lane & 15] = 0u;
        const u32 qoff = WS_QG + h * 16384 + j * 2048 + lane * 16;
        KARG_USE(qoff, qoff);
        LDS_FENCE();
        EPOCH_LOAD();
        RAWBAR();
        if (wave == 7 && s >= 28) {
            __builtin_amdgcn_s_sleep(TLBP - 1);
            u32 c1;
            if (s < 31) {
                const rsrc_t pr = {s == 28 ? kb[4] : s == 29 ? kb[8] : kb[12], s == 28 ? kb[5] : s == 29 ? kb[9] : kb[13], kb[6], kb[7]};
                const u32 pb = s == 28 ? h * 512 * (D * 2) : s == 29 ? h * 224 * (D * 2) : (u32)(h * SK + (SK - WIN)) * (DH * 2);
                const u32 ps = s == 28 ? 73 * (D * 2) : s == 29 ? 31 * (D * 2) : 149792;
                c1 = buf_ld1(pr, lane < 8 ? pb + lane * ps : OOB, 0, AUX_NT);
            } else {
                c1 = 0;
                if (lane < 3) c1 = *mkptr<gu16>(lane == 0 ? kc[0] : lane == 1 ? kc[2] : kc[6], lane == 0 ? kc[1] : lane == 1 ? kc[3] : kc[7]);
            }
            asm volatile("" :: "v"(c1));
        }
        MK_TAG();
        asm volatile("" :: "v"(c0));
        if (wave != 7) {
            u32x4 qa[8];
            const u32 te = now();
            CBAR();
            lds_wait_sleep(&sm.flag[F_PUB], 1);
            __builtin_amdgcn_s_sleep(QDELAY);
            time_wait(te, QT2);
            u32 bad = 1, q0, q1;
#pragma nounroll
            for (u32 spin = 0; spin < SPIN_CAP; ++spin) {
                CBAR();
                if (bad) {
#pragma unroll
                    for (int i = 0; i < 8; ++i) qa[i] = buf_ld4(wsr, qoff + (i & 1) * 1024, (i >> 1) * 4096, AUX_SC1);
                    bad = 0;
#pragma unroll
                    for (int i = 0; i < 8; ++i) bad = tagbad(bad, qa[i], tag);
#define QSUM(j, e) ((fl(qa[j][e]) + fl(qa[2 + j][e])) + (fl(qa[4 + j][e]) + fl(qa[6 + j][e])))
                    q0 = cvt2(QSUM(0, 0), QSUM(0, 2)); q1 = cvt2(QSUM(1, 0), QSUM(1, 2));
                }
                if (__all(bad == 0)) break;
            }
            sm.q[128 * j + lane] = q0; sm.q[128 * j + 64 + lane] = q1;
            LDS_FENCE();
            lds_inc(&sm.flag[F_Q], lane);
        }
    } else {
        const int kw = wave - 4;
        const u32 koff = kvb + kw * KV_HALF + SUBOFF(r16 >> 2) + (4 * g + (r16 & 3)) * 16;
        KARG_USE(koff, koff);
        EPOCH_LOAD();
        RAWBAR();
        __builtin_amdgcn_s_sleep(TLBK - 1);
        const u32 c1 = buf_ld1(ra, (lane < 8 && kw == 0 && s == 31) ? (u32)(h * SK + (SK - WIN)) * (DH * 2) + lane * 149792 : OOB, 0, AUX_NT);
        __builtin_amdgcn_sched_barrier(0);
        u32 so[8];
#pragma unroll
        for (int a = 0; a < 8; ++a) { so[a] = ROTOFF(a); asm volatile("" : "+s"(so[a])); }
        time_wait(t0, KT);
        u32x4 kf[16];
#pragma unroll
        for (int i = 0; i < 16; ++i) {
            kf[i] = ldnt(ra, koff + (i & 1) * 256, so[i >> 1]);
            if (i < 15) PACE(i, KP);
        }
        MK_TAG();
        asm volatile("" :: "v"(c0));
        asm volatile("" :: "v"(c1));
        lds_wait(&sm.flag[F_Q], 2);
        const u32* qp = sm.q + ((r16 >> 2) & 1) * 128 + (4 * g + (r16 & 3)) * 4;
        const u32x4 q0 = *reinterpret_cast<const u32x4*>(qp), q1 = *reinterpret_cast<const u32x4*>(qp + 64);
        float t[8];
#pragma unroll
        for (int a = 0; a < 8; ++a) {
            f32x4 d = {0.f, 0.f, 0.f, 0.f};
            d = mfma(q0, kf[2 * a], d);
            d = mfma(q1, kf[2 * a + 1], d);
            float v = d[a & 3];
            if ((a & 3) != 0) v += dpp<0x00>(d[0]);
            if ((a & 3) != 1) v += dpp<0x55>(d[1]);
            if ((a & 3) != 2) v += dpp<0xaa>(d[2]);
            if ((a & 3) != 3) v += dpp<0xff>(d[3]);
            t[a] = v;
        }
        const bool hi = lane >= 32;
        const float x0 = hi ? t[4] : t[0], x1 = hi ? t[5] : t[1], x2 = hi ? t[6] : t[2], x3 = hi ? t[7] : t[3];
        const float y0 = (lane & 1) ? x1 : x0, y1 = (lane & 1) ? x3 : x2;
        const float z = rbf((lane & 2) ? y1 : y0) * 0.0625f;
        const float m = x32_max(row16_max(z));
        const float e = __builtin_amdgcn_exp2f((z - m) * LOG2E);
        reinterpret_cast<u16*>(sm.p[kw][(r16 >> 2) * 2 + (g & 1)])[4 * (g >> 1) + (lane & 3)] = (u16)cvt2(e, e);
        sm.mx[g & 1][kw ^ ROTH] = m;
        lds_set(&sm.flag[F_P0 + kw]);
        float l = row16_sum(e);
        l = sw32_add(l, l);
        if (r16 < 2) stsc(u32x4{__float_as_uint(m), tag, __float_as_uint(l), tag}, wsr, WS_ST + (h * 64 + 2 * s + kw) * 128 + g * 32 + r16 * 16);
        if (kw) {
            u32 pp = 0;
            if (s < 28) {
                const u32 b1v = mkptr<gu16>(kc[0], kc[1])[UBASE + 8 * (lane & 3) + (lane >> 5)];
                u32x4 ua;
#pragma nounroll
                for (u32 spin = 0; spin < SPIN_CAP; ++spin) {
                    CBAR();
                    ua = ldsc(wsr, WS_U + mi * 1024 + lane * 16);
                    if (__all(tagok(ua, tag))) break;
                    __builtin_amdgcn_s_sleep(RETRY);
                }
                float r = sw32_add(fl(ua[0]), fl(ua[2]));
                r = sw16_add(r, r);
                r += dpp<ROR8>(r); r += dpp<ROR4>(r);
                const float u = rbf(rbf(r) + bflo(b1v));
                const float zz = 0.7978845608028654f * (u + 0.044715f * u * u * u);
                const float th = 1.f - 2.f * __builtin_amdgcn_rcpf(__builtin_amdgcn_exp2f(2.f * LOG2E * zz) + 1.f);
                const float hv = 0.5f * u * (1.f + th);
                pp = cvt2(hv, hv);
            }
            if ((lane & 28) == 0) sm.a[2 * (lane & 3) + (lane >> 5)] = pp;
        } else {
            const u32 soff = WS_ST + (h * 64 + lane) * 128 + (s >> 4) * 32;
            lds_wait_sleep(&sm.flag[F_P1], 1);
            lds_wait_sleep(&sm.flag[F_H], STH);
            time_wait(t0, ST + DSH(SB));
            u32x4 sa;
            u32 bad = 1;
#pragma nounroll
            for (u32 spin = 0; spin < SPIN_CAP; ++spin) {
                CBAR();
                if (bad) { sa = ldsc(wsr, soff); bad = tagbad(0, sa, tag); }
                if (__all(bad == 0)) break;
                __builtin_amdgcn_s_sleep(RETRY);
            }
            const float M = x32_max(x16_max(row16_max(fl(sa[0]))));
            const float e = __builtin_amdgcn_exp2f((fl(sa[0]) - M) * LOG2E);
            const float L = wave_sum(e * fl(sa[2]));
            const float w = pair_max(e) * __builtin_amdgcn_rcpf(L);
            if ((lane & 1) == 0) sm.w[(lane & 30) + (lane >> 5)] = w;
        }
        LDS_FENCE();
        lds_inc(&sm.flag[F_A], lane);
        __builtin_amdgcn_endpgm();
    }
    } else {
        const bool isw1 = wave == 2;
        u32 rg, rstr, gcol;
        asm("s_cmp_eq_u32 %3, 2\n s_cselect_b32 %0, %4, %5\n s_cselect_b32 %1, %6, %7\n s_cselect_b32 %2, %8, %9" : "=&s"(rg), "=&s"(rstr), "=&s"(gcol)
            : "s"(wave), "s"(mi & 7), "s"((s >> 3) * 2 + wave), "s"(F * 16), "s"(NQ * 16), "s"(mi >> 3), "s"(h * 8 + (s & 7)) : "scc");
        rg *= 7;
        const u32 glim = wave < (s < 28 ? 3 : 2) ? 56 : 0;
        const bool gon = glim != 0;
        u32 goff = __umul24(rg + (lane >> 3), rstr) + (gcol * 8 + (lane & 7)) * 16;
        goff = lane < glim ? goff : OOB;
        const u32 xoff = lane * 16;
        const u32 poff = (lane == 0 && wave == 1) ? goff : OOB;
        asm volatile("" :: "v"(poff));
        KARG_USE(goff, xoff);
        const u32 c1 = buf_ld1(ra, poff, 0, AUX_NT);
        __builtin_amdgcn_sched_barrier(0);
        const u32x4 xv = buf_ld4(rb, xoff, 0, 0);
        if (isw1) { RAWBAR(); time_wait(t0, W1T); }
        u32x4 wf[8];
        wf[0] = ldnt(ra, goff, 0);
#pragma unroll
        for (int i = 1; i < 8; ++i) wf[i] = ldnt(ra, goff, ka[7 + i]);
        EPOCH_LOAD();
        u32 sel[2][4];
#pragma unroll
        for (int d = 0; d < 4; ++d) {
            const bool on = ((lane & 7) >> 1) == d;
            sel[0][d] = on ? ((lane & 1) ? 0x01000c0cu : 0x0c0c0100u) : 0x0c0c0c0cu;
            sel[1][d] = on ? ((lane & 1) ? 0x03020c0cu : 0x0c0c0302u) : 0x0c0c0c0cu;
            asm volatile("" : "+v"(sel[0][d]), "+v"(sel[1][d]));
        }
        if (!isw1) RAWBAR();
        u32 xp[4] = {xv[0], xv[1], xv[2], xv[3]};
#pragma nounroll
        for (int it = isw1 ? 0 : 1; it < 2; ++it) {
            float v[8];
#pragma unroll
            for (int d = 0; d < 4; ++d) { v[2 * d] = bflo(xp[d]); v[2 * d + 1] = bfhi(xp[d]); }
            const float c = rdl(v[0], 0);
            float s1 = 0.f, s2 = 0.f;
#pragma unroll
            for (int j = 0; j < 8; ++j) { const float d = v[j] - c; s1 += d; s2 = fmaf(d, d, s2); }
            if (lane >= 56) { s1 = 0.f; s2 = 0.f; }
            float r = row16_sum(sw32_add(s1, s2));
            r = sw16_add(r, r) * (1.f / D);
            const float md = rdl(r, 0), var = fmaxf(fmaf(-md, md, rdl(r, 32)), 0.f);
            const float mb = rbf(c + md);
            const float rden = __builtin_amdgcn_rcpf(rbf(__builtin_amdgcn_sqrtf(rbf(rbf(var) + 1e-5f))));
#pragma unroll
            for (int d = 0; d < 4; ++d) {
                const u32 t = cvt2(v[2 * d] - mb, v[2 * d + 1] - mb);
                xp[d] = cvt2(bflo(t) * rden, bfhi(t) * rden);
            }
        }
        u32 yp[4];
#pragma unroll
        for (int d = 0; d < 4; ++d) yp[d] = __builtin_amdgcn_ds_bpermute((rg + (lane >> 3)) * 4, xp[d]);
        MK_TAG();
        f32x4 acc = {0.f, 0.f, 0.f, 0.f};
#pragma unroll
        for (int i = 0; i < 8; ++i) acc = mfma(perm4(yp[i >> 1], sel[i & 1]), wf[i], acc);
        asm volatile("" :: "v"(c0));
        asm volatile("" :: "v"(c1));
        const bool vl = ((lane >> 3) & 1) == (lane >> 5);
        if (wave < 2) {
            if (vl) *reinterpret_cast<f32x4*>(&sm.gp[2 * wave + (g >> 1)][8 * (lane & 7) + 4 * (g & 1)]) = acc;
            LDS_FENCE();
            u32 old = 0;
            if (lane == 0) old = __hip_atomic_fetch_add((lds_u32*)&sm.flag[F_G], 1u, __ATOMIC_RELAXED, __HIP_MEMORY_SCOPE_WORKGROUP);
            if (__builtin_amdgcn_readfirstlane(old) == 1) {
                CBAR();
                const int ci = 2 * (lane & 31);
                const f32x2 r = (*reinterpret_cast<const f32x2*>(&sm.gp[0][ci]) + *reinterpret_cast<const f32x2*>(&sm.gp[1][ci])) +
                                (*reinterpret_cast<const f32x2*>(&sm.gp[2][ci]) + *reinterpret_cast<const f32x2*>(&sm.gp[3][ci]));
                if (lane < 32) stsc(u32x4{__float_as_uint(r[0]), tag, __float_as_uint(r[1]), tag}, wsr, WS_QG + (h * 32 + s) * 512 + lane * 16);
                CBAR();
                u32 d = now() - t0;
                d = d > EN ? d - EN : 0;
                d = d < DCAP ? d : DCAP;
                *(volatile lds_u32*)&sm.flag[F_PUB] = d + 1;
            }
            const rsrc_t vr = {kb[12], kb[13], kb[14], kb[15]};
            const int rh = ROTH;
            const u32 voff = kvb + rh * KV_HALF + wave * 256 + SUBOFF(g) + r16 * 16, voff1 = voff ^ KV_HALF;
            u32 so[8];
#pragma unroll
            for (int a = 0; a < 8; ++a) { so[a] = ROTOFF(a); asm volatile("" : "+s"(so[a])); }
            const u32 hoff = H2OFF(wave);
            asm volatile("" :: "v"(voff), "v"(voff1), "v"(hoff));
            u32x4 vf[16];
            stsc(u32x4{0u, ~tag, 0u, ~tag}, wsr, hoff);
            time_wait(t0, VT + DSH(SA));
#pragma unroll
            for (int t = 0; t < 16; ++t) {
                vf[t] = ldnt(vr, t < 8 ? voff : voff1, so[t & 7]);
                if (t < 15) PACE(t, VP);
            }
            f32x4 o = {0.f, 0.f, 0.f, 0.f}, o1 = o;
            {
                lds_wait(&sm.flag[F_P0 + rh], 1);
                const u32x4 pv = *reinterpret_cast<const u32x4*>(sm.p[rh][lane >> 3]);
#pragma unroll
                for (int t = 0; t < 8; ++t) o = mfma(perm4(pv[t >> 1], sel[t & 1]), vf[t], o);
                asm volatile("" : "+v"(o));
            }
            lds_wait(&sm.flag[F_P0 + (1 ^ rh)], 1);
            const f32x2 mm = *reinterpret_cast<const f32x2*>(sm.mx[g >> 1]);
            const u32x4 pv = *reinterpret_cast<const u32x4*>(sm.p[1 ^ rh][lane >> 3]);
            __builtin_amdgcn_sched_barrier(0);
            const float mw = fmx(mm[0], mm[1]);
            float al0 = __builtin_amdgcn_exp2f((mm[0] - mw) * LOG2E), al1 = __builtin_amdgcn_exp2f((mm[1] - mw) * LOG2E);
            o *= al0;
            asm volatile("" : "+v"(o), "+v"(al1));
            u32x4 af[8];
#pragma unroll
            for (int t = 0; t < 8; ++t) { af[t] = perm4(pv[t >> 1], sel[t & 1]); asm volatile("" : "+v"(af[t])); }
#pragma unroll
            for (int t = 0; t < 8; ++t) o1 = mfma(af[t], vf[8 + t], o1);
            o += al1 * o1;
            stsc(u32x4{cvt2(o[0], o[1]), tag, cvt2(o[2], o[3]), tag}, wsr, hoff);
            CBAR();
            lds_inc(&sm.flag[F_H], lane);
            __builtin_amdgcn_endpgm();
        }
        if (gon) {
            const float p2 = dpp<ROR8>(acc[2]), p3 = dpp<ROR8>(acc[3]);
            const float u0 = vl ? acc[0] : p2, u1 = vl ? acc[1] : p3;
            stsc(u32x4{__float_as_uint(u0), tag, __float_as_uint(u1), tag}, wsr,
                 WS_U + (((mi & ~7u) + (lane & 4) + (g & 1) * 2 + (vl ? 0 : 1)) * 16 + (mi & 7) * 2 + (g >> 1)) * 64 + (lane & 3) * 16);
        }
    }
    {
        const int T = (wave & 1) + (wave >> 2) * 2;
        const rsrc_t wor = {kb[4], kb[5], kb[6], kb[7]}, w2r = {kb[8], kb[9], kb[10], kb[11]};
        const u32 coff = (16 * T + r16 < 56) ? (u32)(128 * T + 8 * r16) * 2 : OOB;
        const u32 o2 = s < 28 ? coff + (UBASE + 8 * g) * (D * 2) : OOB, oo = coff + (h * 512 + (s >> 1) * 32 + (s & 1) * 4 + g) * (D * 2);
        const u32 doff = H2ADDR(s, lane >> 2) + (lane & 3) * 16;
        const u32 h3off = H3OFF(T);
        u32 mk[4];
#pragma unroll
        for (int d = 0; d < 4; ++d) mk[d] = (((lane & 7) >> 1) == d) ? ((lane & 1) ? 0xffff0000u : 0x0000ffffu) : 0u;
        asm volatile("" :: "v"(o2), "v"(oo), "v"(doff), "v"(h3off), "v"(mk[0]), "v"(mk[1]), "v"(mk[2]), "v"(mk[3]));
        stsc(u32x4{0u, ~tag, 0u, ~tag}, wsr, h3off);
        time_wait(t0, TAILT + DSH(SC));
        const u32 h2t = H2T + DSH(SB);
        u32x4 tf[6];
        tf[4] = ldnt(w2r, o2, 0); PACE(0, TP); tf[5] = ldnt(w2r, o2 + D * 2, 0);
#pragma unroll
        for (int t = 0; t < 4; ++t) { PACE(t + 1, TP); tf[t] = ldnt(wor, oo, t * 8 * D * 2); }
        lds_wait_sleep(&sm.flag[F_H], 2);
        time_wait(now(), HC);
        time_wait(t0, h2t);
        u32x4 da = ldsc(wsr, doff), db = ldsc(wsr, doff + H2STEP);
        lds_wait(&sm.flag[F_A], 2);
        const u32x2 ah = *reinterpret_cast<const u32x2*>(&sm.a[2 * g]);
        const f32x2 wv = *reinterpret_cast<const f32x2*>(&sm.w[2 * (lane >> 2)]);
        f32x4 acc = {0.f, 0.f, 0.f, 0.f};
        acc = mfma(mask4(ah[0], mk), tf[4], acc); acc = mfma(mask4(ah[1], mk), tf[5], acc);
        asm volatile("" : "+v"(acc) : "v"(wv));
        u32 bad;
        __builtin_amdgcn_sched_barrier(0);
        bad = tagbad(tagbad(0, da, tag), db, tag);
        if (!__all(bad == 0)) {
#pragma nounroll
            for (u32 spin = 0; spin < SPIN_CAP; ++spin) {
                CBAR();
                if (bad) { da = ldsc(wsr, doff); db = ldsc(wsr, doff + H2STEP); bad = tagbad(tagbad(0, da, tag), db, tag); }
                if (__all(bad == 0)) break;
                if (RETRY) __builtin_amdgcn_s_sleep(RETRY);
            }
        }
        const float v0 = fmaf(wv[1], bflo(db[0]), wv[0] * bflo(da[0])), v1 = fmaf(wv[1], bfhi(db[0]), wv[0] * bfhi(da[0]));
        const float v2 = fmaf(wv[1], bflo(db[2]), wv[0] * bflo(da[2])), v3 = fmaf(wv[1], bfhi(db[2]), wv[0] * bfhi(da[2]));
        float v = sw16_add(sw32_add(v0, v2), sw32_add(v1, v3));
        v += dpp<ROR8>(v); v += dpp<ROR4>(v);
        const u32 ov = cvt2(v, v);
        acc = mfma(mask4(dppu<0x00>(ov), mk), tf[0], acc);
        acc = mfma(mask4(dppu<0x55>(ov), mk), tf[1], acc);
        acc = mfma(mask4(dppu<0xaa>(ov), mk), tf[2], acc);
        acc = mfma(mask4(dppu<0xff>(ov), mk), tf[3], acc);
        {
            const float c0 = g < 2 ? acc[0] : acc[2], c1 = g < 2 ? acc[1] : acc[3];
            stsc(u32x4{__float_as_uint(c0), tag, __float_as_uint(c1), tag}, wsr, h3off);
        }
        if (s >= 24 && h < 7) {
            const int cg = h * 8 + s - 24;
            const u32 col = (cg >> 2) * 32 + (cg & 1) * 4 + (cg & 2) + 8 * ((lane >> 1) & 3) + (lane & 1);
            const u32 xr = mkptr<gu16>(kc[4], kc[5])[col], b2r = mkptr<gu16>(kc[2], kc[3])[col];
            const u32 off = WS_H3 + ((cg >> 1) * 256 + T * 64 + (lane >> 2)) * 128 + (cg & 1) * 64 + (lane & 3) * 16;
            __builtin_amdgcn_s_sleep(H3DELAY);
            float c0, c1;
            u32 bad = 1;
#pragma nounroll
            for (u32 spin = 0; spin < SPIN_CAP; ++spin) {
                CBAR();
                if (bad) {
                    const u32x4 ga = ldsc(wsr, off), gb = ldsc(wsr, off + 2048), gc = buf_ld4(wsr, off, 4096, AUX_SC1), gd = buf_ld4(wsr, off + 2048, 4096, AUX_SC1);
                    bad = tagbad(tagbad(tagbad(tagbad(0, ga, tag), gb, tag), gc, tag), gd, tag);
                    c0 = (fl(ga[0]) + fl(gb[0])) + (fl(gc[0]) + fl(gd[0]));
                    c1 = (fl(ga[2]) + fl(gb[2])) + (fl(gc[2]) + fl(gd[2]));
                }
                if (__all(bad == 0)) break;
                __builtin_amdgcn_s_sleep(RETRY);
            }
            float c = sw32_add(c0, c1);
            c = sw16_add(c, c);
            c += dpp<ROR8>(c); c += dpp<ROR4>(c);
            if ((lane & 28) == 0) sm.fin[T][2 * (lane & 3) + (lane >> 5)] = c;
            BARRIER();
            if (wave == 2 && lane < 8) {
                const float r = (bflo(xr) + bflo(b2r)) + ((sm.fin[0][lane] + sm.fin[1][lane]) + (sm.fin[2][lane] + sm.fin[3][lane]));
                mkptr<__attribute__((address_space(1))) u16>(kc[6], kc[7])[col] = (u16)(cvt2(r, r) >> 16);
                if (cg == 0 && lane == 0)
                    __hip_atomic_store(mkptr<__attribute__((address_space(1))) u32>(kb[0], kb[1]) + WS_EPOCH / 4, ep1, __ATOMIC_RELAXED, __HIP_MEMORY_SCOPE_AGENT);
            }
        }
    }
}

extern "C" __device__ __attribute__((used, noinline)) void fused_decode_end() {}

extern "C" void kernel_launch(void* const* d_in, const int* in_sizes, int n_in,
                              void* d_out, int out_size, void* d_ws, size_t ws_size,
                              hipStream_t stream) {
    const u32 FL = 0x00020000u, BIG = 0x7fffffffu;
    Args a;
    const void* pa[3] = {d_in[3], d_in[6], d_in[1]};
    const u32 st[3] = {NQ * 2, F * 2, 2048};
    for (int r = 0; r < 3; ++r) {
        a.r[r].a = Desc{pa[r], BIG, FL};
        a.r[r].b = Desc{d_in[0], (u32)(D * 2), FL};
        for (int i = 0; i < 8; ++i) a.r[r].so[i] = (i + 1) * st[r];
    }
    a.ws = Desc{d_ws, WS_END, FL};
    a.wo = Desc{d_in[4], BIG, FL};
    a.w2 = Desc{d_in[8], BIG, FL};
    a.v = Desc{d_in[2], BIG, FL};
    a.b1 = (const u16*)d_in[7]; a.b2 = (const u16*)d_in[9]; a.x = (const u16*)d_in[0]; a.out = (u16*)d_out;
    fused_decode<<<NWG, NT, 0, stream>>>(a);
}
```
